```python
import torch

def _softmax(x, axis=-1):
    return torch.softmax(x, dim=axis)
def _gelu(x):
    return torch.nn.functional.gelu(x, approximate="tanh")
def _silu(x):
    return torch.nn.functional.silu(x)
def _sigmoid(x):
    return torch.sigmoid(x)
def _layernorm(x, axis=-1, eps=1e-5):
    m = x.mean(dim=axis, keepdim=True)
    v = x.var(dim=axis, keepdim=True, unbiased=False)
    return (x - m) / torch.sqrt(v + eps)
def _scatter_add(tgt, idx, src, axis=0):
    return tgt.clone().index_add_(axis, idx, src)
def _segment_sum(src, seg, n):
    out = torch.zeros((n,) + src.shape[1:], dtype=src.dtype)
    return out.index_add_(0, seg, src)
def _pad(x, pw):
    flat = tuple(v for pair in reversed(pw) for v in pair)
    return torch.nn.functional.pad(x, flat)

kernel_name = 'standalone__weight_only_int_omniquant__prompt'

def setup_inputs():

    _w = (lambda a: (lambda t: t.to(torch.bfloat16) if t.is_floating_point() else t)(torch.as_tensor(a)))
    B, S, D, F = 1, 2048, 1024, 3584
    bits = 3
    G = 64 if 64 > 0 else D
    D = (D // G) * G
    ng = D // G
    qmax = (1 << bits) - 1
    qw = torch.randint(0, qmax + 1, (D, F)).to(torch.int32)
    scales = (torch.abs((lambda *s: torch.randn(*s, dtype=torch.bfloat16) * 0.05)(ng, F)) * 0.05 + 0.01).to(float)
    zeros = torch.randint(0, qmax + 1, (ng, F)).to(float)
    act_scale = (torch.abs((lambda *s: torch.randn(*s, dtype=torch.bfloat16) * 0.05)(D)) * 0.5 + 0.5).to(float)
    return {
        "x": _w((lambda *s: torch.randn(*s, dtype=torch.bfloat16) * 0.05)(B, S, D)),
        "qw": _w(qw),
        "scales": _w(scales),
        "zeros": _w(zeros),
        "act_scale": _w(act_scale),
    }

def reference(x, qw, scales, zeros, act_scale):
    B, S, D = x.shape
    ng, F = scales.shape
    G = D // ng
    s = (lambda x, n, axis: torch.repeat_interleave(x, n, dim=axis))(scales, G, axis=0)
    z = (lambda x, n, axis: torch.repeat_interleave(x, n, dim=axis))(zeros, G, axis=0)
    w = ((lambda x, dt: x.to(dt))(qw, x.dtype) - z) * s
    xs = x * (lambda x, dt: x.to(dt))(act_scale, x.dtype)
    return torch.einsum("bsd,df->bsf", xs, w)
```

```cpp
#include <hip/hip_runtime.h>
__global__ __launch_bounds__(512, 2) void wq_gemm(const void* x, const void* qw, const void* sc, const void* zr, const void* as, void* out) {
  unsigned tid = threadIdx.x;
  asm volatile(
      "L_start_%=:\n"
      "s_load_dwordx8 s[8:15], %[ka], 0\n"
      "s_load_dwordx4 s[16:19], %[ka], 32\n"
      "v_readfirstlane_b32 s4, %[tid]\n"
      "v_and_b32 v1, 63, %[tid]\n"
      "s_and_b32 s36, %[bid], 7\n"
      "s_lshr_b32 s40, %[bid], 3\n"
      "s_lshr_b32 s4, s4, 6\n"
      "s_cmp_lt_u32 s40, 4\n"
      "s_cbranch_scc1 L_help_%=\n"
      "s_sub_u32 s40, s40, 4\n"
      "s_mul_i32 s41, s40, 37\n"
      "s_lshr_b32 s41, s41, 8\n"
      "s_mul_i32 s42, s41, 7\n"
      "s_sub_u32 s42, s40, s42\n"
      "s_and_b32 s43, s36, 1\n"
      "s_lshl2_add_u32 s5, s43, s41\n"
      "s_lshr_b32 s43, s36, 1\n"
      "s_mul_i32 s43, s43, 7\n"
      "s_add_u32 s34, s43, s42\n"
      "s_mov_b32 s47, s41\n"
      "s_add_u32 s41, s41, s42\n"
      "s_bitcmp1_b32 s41, 0\n"
      "s_mov_b32 s48, 0x75634210\n"
      "s_cmov_b32 s48, 0x67453201\n"
      "s_mov_b32 s49, 0xfedcab89\n"
      "s_cmov_b32 s49, 0xfedcb9a8\n"
      "s_mov_b32 s44, 0x76543120\n"
      "s_cmov_b32 s44, 0x86542310\n"
      "s_mov_b32 s45, 0xfedcba98\n"
      "s_cmov_b32 s45, 0xfedbca97\n"
      "s_bitcmp1_b32 s41, 1\n"
      "s_cmov_b64 s[48:49], s[44:45]\n"
      "s_and_b32 s35, s4, 3\n"
      "s_lshl_b32 s35, s35, 13\n"
      "s_mov_b32 s37, 0x4b800000\n"
      "s_mov_b32 s38, 0xbf800000\n"
      "s_mov_b32 s39, 0x3f800000\n"
      "s_cmp_lt_u32 s4, 4\n"
      "s_cbranch_scc1 L_prod_%=\n"
      "s_and_b32 s40, s4, 3\n"
      "v_lshl_add_u32 v5, s40, 6, v1\n"
      "v_lshrrev_b32 v6, 4, v5\n"
      "v_and_b32 v28, 15, v5\n"
      "s_lshl_b32 s40, s34, 8\n"
      "v_lshl_add_u32 v29, v28, 4, s40\n"
      "s_movk_i32 s40, 7168\n"
      "v_mad_u32_u24 v29, v6, s40, v29\n"
      "v_lshlrev_b32 v30, 3, v5\n"
      "s_waitcnt lgkmcnt(0)\n"
      "global_load_dwordx4 v[32:35], v29, s[12:13]\n"
      "global_load_dwordx4 v[36:39], v29, s[14:15]\n"
      "global_load_dwordx2 v[40:41], v30, s[16:17]\n"
      "s_branch L_cons_%=\n"
      "L_help_%=:\n"
      "s_getpc_b64 s[60:61]\n"
      "L_hpc_%=:\n"
      "s_lshl3_add_u32 s41, s40, s4\n"
      "s_sub_u32 s60, s60, L_hpc_%=-L_start_%=\n"
      "s_subb_u32 s61, s61, 0\n"
      "s_and_b32 s42, s41, 1\n"
      "v_lshl_add_u32 v12, s42, 6, v1\n"
      "v_lshlrev_b32 v12, 7, v12\n"
      "v_min_u32 v12, L_end_%=-L_start_%=-4, v12\n"
      "s_cmp_lt_u32 s41, 2\n"
      "s_cbranch_scc0 L_hnopf_%=\n"
      "global_load_dword v3, v12, s[60:61]\n"
      "L_hnopf_%=:\n"
      "s_lshl3_add_u32 s41, s40, s4\n"
      "s_mul_i32 s41, s41, 36\n"
      "v_add_u32 v2, s41, v1\n"
      "v_mul_u32_u24 v4, 2341, v2\n"
      "v_lshrrev_b32 v4, 16, v4\n"
      "v_mul_u32_u24 v5, 28, v4\n"
      "v_sub_u32 v5, v2, v5\n"
      "s_and_b32 s42, s36, 1\n"
      "s_lshr_b32 s43, s36, 1\n"
      "s_lshl_b32 s46, s42, 5\n"
      "s_add_u32 s46, s46, 192\n"
      "s_mul_i32 s46, s46, 14336\n"
      "s_mul_i32 s47, s43, 3584\n"
      "s_add_u32 s46, s46, s47\n"
      "s_movk_i32 s47, 14336\n"
      "v_lshl_add_u32 v5, v5, 7, s46\n"
      "v_mad_u32_u24 v5, v4, s47, v5\n"
      "s_lshl2_add_u32 s46, s42, s43\n"
      "s_lshl_b32 s46, s46, 19\n"
      "s_add_u32 s46, s46, 384\n"
      "v_subrev_u32 v6, 896, v2\n"
      "v_lshl_add_u32 v6, v6, 11, s46\n"
      "v_cmp_gt_u32 s[46:47], 36, v1\n"
      "v_cmp_gt_u32 vcc, 896, v2\n"
      "s_waitcnt lgkmcnt(0)\n"
      "s_mov_b32 s24, s10\n"
      "s_and_b32 s25, s11, 0xffff\n"
      "s_mov_b32 s26, 14680064\n"
      "s_mov_b32 s27, 0x20000\n"
      "s_mov_b32 s20, s8\n"
      "s_and_b32 s21, s9, 0xffff\n"
      "s_mov_b32 s22, 4194304\n"
      "s_mov_b32 s23, 0x20000\n"
      "s_andn2_b64 s[44:45], s[46:47], vcc\n"
      "s_and_b64 s[46:47], s[46:47], vcc\n"
      "s_mov_b32 s42, 0\n"
      "s_mov_b32 s43, 0\n"
      "s_sleep 20\n"
      "L_hloop_%=:\n"
      "s_mov_b64 exec, s[46:47]\n"
      "s_cbranch_execz L_hskipw_%=\n"
      "buffer_load_dword v8, v5, s[24:27], s42 offen\n"
      "L_hskipw_%=:\n"
      "s_mov_b64 exec, s[44:45]\n"
      "s_cbranch_execz L_hskipx_%=\n"
      "buffer_load_dword v9, v6, s[20:23], s43 offen\n"
      "L_hskipx_%=:\n"
      "s_add_u32 s42, s42, 917504\n"
      "s_add_u32 s43, s43, 128\n"
      "s_cmp_lt_u32 s43, 1664\n"
      "s_waitcnt vmcnt(0)\n"
      "s_cbranch_scc1 L_hloop_%=\n"
      "s_endpgm\n"
      "L_prod_%=:\n"
      "v_lshrrev_b32 v2, 3, v1\n"
      "v_and_b32 v3, 7, v1\n"
      "s_and_b32 s42, s48, 15\n"
      "s_lshl2_add_u32 s40, s34, s4\n"
      "s_mul_i32 s44, s42, 7168\n"
      "s_lshl_b32 s45, s40, 6\n"
      "s_add_u32 s44, s44, s45\n"
      "v_lshl_add_u32 v4, v3, 3, s44\n"
      "s_lshl_b32 s44, s42, 7\n"
      "v_lshl_add_u32 v5, v2, 4, s44\n"
      "s_lshl_b32 s40, s40, 7\n"
      "v_lshl_add_u32 v144, v3, 4, s40\n"
      "s_mov_b32 s44, 114688\n"
      "v_mad_u32_u24 v144, v2, s44, v144\n"
      "s_mul_i32 s43, s42, 917504\n"
      "s_waitcnt lgkmcnt(0)\n"
      "global_load_dwordx2 v[8:9], v4, s[12:13]\n"
      "global_load_dwordx2 v[10:11], v4, s[14:15]\n"
      "global_load_dwordx4 v[12:15], v5, s[16:17]\n"
      "s_mov_b32 s24, s10\n"
      "s_and_b32 s25, s11, 0xffff\n"
      "s_mov_b32 s26, 14680064\n"
      "s_mov_b32 s27, 0x20000\n"
      "s_mov_b32 s27, 0x74fac\n"
      "buffer_load_format_d16_xyzw v[32:33], v144, s[24:27], s43 offen\n"
      "v_add_u32 v145, 14336, v144\n"
      "buffer_load_format_d16_xyzw v[36:37], v145, s[24:27], s43 offen\n"
      "v_add_u32 v146, 28672, v144\n"
      "buffer_load_format_d16_xyzw v[40:41], v146, s[24:27], s43 offen\n"
      "v_add_u32 v147, 43008, v144\n"
      "buffer_load_format_d16_xyzw v[44:45], v147, s[24:27], s43 offen\n"
      "v_add_u32 v148, 57344, v144\n"
      "buffer_load_format_d16_xyzw v[48:49], v148, s[24:27], s43 offen\n"
      "v_add_u32 v149, 71680, v144\n"
      "buffer_load_format_d16_xyzw v[52:53], v149, s[24:27], s43 offen\n"
      "v_add_u32 v150, 86016, v144\n"
      "buffer_load_format_d16_xyzw v[56:57], v150, s[24:27], s43 offen\n"
      "v_add_u32 v151, 100352, v144\n"
      "buffer_load_format_d16_xyzw v[60:61], v151, s[24:27], s43 offen\n"
      "s_mov_b32 s20, s8\n"
      "s_and_b32 s21, s9, 0xffff\n"
      "s_mov_b32 s22, 4194304\n"
      "s_mov_b32 s23, 0x20000\n"
      "s_lshl_b32 s40, s5, 19\n"
      "v_lshrrev_b32 v16, 4, v1\n"
      "v_xor_b32 v16, v16, v1\n"
      "v_and_b32 v16, 7, v16\n"
      "v_lshrrev_b32 v17, 3, v1\n"
      "v_lshlrev_b32 v17, 11, v17\n"
      "v_lshl_add_u32 v16, v16, 4, v17\n"
      "v_add_u32 v152, s40, v16\n"
      "v_xor_b32 v153, 64, v152\n"
      "v_add_u32 v154, 30720, v152\n"
      "v_add_u32 v155, 46080, v153\n"
      "v_add_u32 v153, 15360, v153\n"
      "s_lshl_b32 s40, s4, 7\n"
      "s_add_u32 s40, s40, 135168\n"
      "v_lshl_add_u32 v159, v3, 4, s40\n"
      "v_lshlrev_b32 v160, 5, v2\n"
      "v_add_u32 v160, 131072, v160\n"
      "s_lshr_b32 s40, s4, 1\n"
      "s_mul_i32 s40, s40, 6\n"
      "s_add_u32 s40, s40, s4\n"
      "s_lshl_b32 s40, s40, 10\n"
      "s_add_u32 s40, s40, 98304\n"
      "v_lshl_add_u32 v158, v3, 7, s40\n"
      "v_xor_b32 v16, v2, v3\n"
      "v_lshl_add_u32 v158, v16, 4, v158\n"
      "s_lshr_b32 s40, s35, 10\n"
      "s_lshr_b32 s44, 0x14181c1c, s40\n"
      "s_lshr_b32 s44, s49, s44\n"
      "s_and_b32 s44, s44, 15\n"
      "s_lshl_b32 s44, s44, 7\n"
      "s_lshr_b32 s45, 0x3030302, s40\n"
      "s_and_b32 s45, s45, 3\n"
      "s_lshl_b32 s45, s45, 16\n"
      "s_add_u32 s44, s44, s45\n"
      "s_add_u32 s45, s35, 0\n"
      "s_mov_b32 m0, s45\n"
      "s_lshr_b64 s[48:49], s[48:49], 4\n"
      "s_and_b32 s41, s48, 15\n"
      "s_mul_i32 s43, s41, 917504\n"
      "s_mov_b32 s46, 0\n"
      "s_branch L_stubp_%=\n"
      "L_backp_%=:\n"
      "s_waitcnt vmcnt(8)\n"
      "v_lshlrev_b32 v96, 16, v8\n"
      "v_and_b32 v97, 0xffff0000, v8\n"
      "v_lshlrev_b32 v98, 16, v9\n"
      "v_and_b32 v99, 0xffff0000, v9\n"
      "v_lshlrev_b32 v100, 16, v10\n"
      "v_and_b32 v101, 0xffff0000, v10\n"
      "v_lshlrev_b32 v102, 16, v11\n"
      "v_and_b32 v103, 0xffff0000, v11\n"
      "v_lshlrev_b32 v104, 16, v12\n"
      "v_and_b32 v105, 0xffff0000, v12\n"
      "v_lshlrev_b32 v106, 16, v13\n"
      "v_and_b32 v107, 0xffff0000, v13\n"
      "v_lshlrev_b32 v108, 16, v14\n"
      "v_and_b32 v109, 0xffff0000, v14\n"
      "v_lshlrev_b32 v110, 16, v15\n"
      "v_and_b32 v111, 0xffff0000, v15\n"
      "v_mul_f32 v100, v100, v96\n"
      "v_mul_f32 v101, v101, v97\n"
      "v_mul_f32 v102, v102, v98\n"
      "v_mul_f32 v103, v103, v99\n"
      "v_mul_f32 v100, s38, v100\n"
      "v_mul_f32 v101, s38, v101\n"
      "v_mul_f32 v102, s38, v102\n"
      "v_mul_f32 v103, s38, v103\n"
      "v_mul_f32 v96, s37, v96\n"
      "v_mul_f32 v97, s37, v97\n"
      "v_mul_f32 v98, s37, v98\n"
      "v_mul_f32 v99, s37, v99\n"
      "s_waitcnt vmcnt(6)\n"
      "v_fma_mix_f32 v168, v32, v96, v100 op_sel:[0,0,0] op_sel_hi:[1,0,0]\n"
      "v_fma_mix_f32 v169, v32, v97, v101 op_sel:[1,0,0] op_sel_hi:[1,0,0]\n"
      "v_fma_mix_f32 v170, v33, v98, v102 op_sel:[0,0,0] op_sel_hi:[1,0,0]\n"
      "v_fma_mix_f32 v171, v33, v99, v103 op_sel:[1,0,0] op_sel_hi:[1,0,0]\n"
      "v_fma_mix_f32 v172, v36, v96, v100 op_sel:[0,0,0] op_sel_hi:[1,0,0]\n"
      "v_fma_mix_f32 v173, v36, v97, v101 op_sel:[1,0,0] op_sel_hi:[1,0,0]\n"
      "v_fma_mix_f32 v174, v37, v98, v102 op_sel:[0,0,0] op_sel_hi:[1,0,0]\n"
      "v_fma_mix_f32 v175, v37, v99, v103 op_sel:[1,0,0] op_sel_hi:[1,0,0]\n"
      "v_mul_f32 v168, v104, v168\n"
      "v_mul_f32 v169, v104, v169\n"
      "v_mul_f32 v170, v104, v170\n"
      "v_mul_f32 v171, v104, v171\n"
      "v_mul_f32 v172, v105, v172\n"
      "v_mul_f32 v173, v105, v173\n"
      "v_mul_f32 v174, v105, v174\n"
      "v_mul_f32 v175, v105, v175\n"
      "v_cvt_pk_bf16_f32 v128, v168, v172\n"
      "v_cvt_pk_bf16_f32 v132, v169, v173\n"
      "v_cvt_pk_bf16_f32 v136, v170, v174\n"
      "v_cvt_pk_bf16_f32 v140, v171, v175\n"
      "s_waitcnt vmcnt(4)\n"
      "v_fma_mix_f32 v176, v40, v96, v100 op_sel:[0,0,0] op_sel_hi:[1,0,0]\n"
      "v_fma_mix_f32 v177, v40, v97, v101 op_sel:[1,0,0] op_sel_hi:[1,0,0]\n"
      "v_fma_mix_f32 v178, v41, v98, v102 op_sel:[0,0,0] op_sel_hi:[1,0,0]\n"
      "v_fma_mix_f32 v179, v41, v99, v103 op_sel:[1,0,0] op_sel_hi:[1,0,0]\n"
      "v_fma_mix_f32 v180, v44, v96, v100 op_sel:[0,0,0] op_sel_hi:[1,0,0]\n"
      "v_fma_mix_f32 v181, v44, v97, v101 op_sel:[1,0,0] op_sel_hi:[1,0,0]\n"
      "v_fma_mix_f32 v182, v45, v98, v102 op_sel:[0,0,0] op_sel_hi:[1,0,0]\n"
      "v_fma_mix_f32 v183, v45, v99, v103 op_sel:[1,0,0] op_sel_hi:[1,0,0]\n"
      "v_mul_f32 v176, v106, v176\n"
      "v_mul_f32 v177, v106, v177\n"
      "v_mul_f32 v178, v106, v178\n"
      "v_mul_f32 v179, v106, v179\n"
      "v_mul_f32 v180, v107, v180\n"
      "v_mul_f32 v181, v107, v181\n"
      "v_mul_f32 v182, v107, v182\n"
      "v_mul_f32 v183, v107, v183\n"
      "v_cvt_pk_bf16_f32 v129, v176, v180\n"
      "v_cvt_pk_bf16_f32 v133, v177, v181\n"
      "v_cvt_pk_bf16_f32 v137, v178, v182\n"
      "v_cvt_pk_bf16_f32 v141, v179, v183\n"
      "s_waitcnt vmcnt(2)\n"
      "v_fma_mix_f32 v168, v48, v96, v100 op_sel:[0,0,0] op_sel_hi:[1,0,0]\n"
      "v_fma_mix_f32 v169, v48, v97, v101 op_sel:[1,0,0] op_sel_hi:[1,0,0]\n"
      "v_fma_mix_f32 v170, v49, v98, v102 op_sel:[0,0,0] op_sel_hi:[1,0,0]\n"
      "v_fma_mix_f32 v171, v49, v99, v103 op_sel:[1,0,0] op_sel_hi:[1,0,0]\n"
      "v_fma_mix_f32 v172, v52, v96, v100 op_sel:[0,0,0] op_sel_hi:[1,0,0]\n"
      "v_fma_mix_f32 v173, v52, v97, v101 op_sel:[1,0,0] op_sel_hi:[1,0,0]\n"
      "v_fma_mix_f32 v174, v53, v98, v102 op_sel:[0,0,0] op_sel_hi:[1,0,0]\n"
      "v_fma_mix_f32 v175, v53, v99, v103 op_sel:[1,0,0] op_sel_hi:[1,0,0]\n"
      "v_mul_f32 v168, v108, v168\n"
      "v_mul_f32 v169, v108, v169\n"
      "v_mul_f32 v170, v108, v170\n"
      "v_mul_f32 v171, v108, v171\n"
      "v_mul_f32 v172, v109, v172\n"
      "v_mul_f32 v173, v109, v173\n"
      "v_mul_f32 v174, v109, v174\n"
      "v_mul_f32 v175, v109, v175\n"
      "v_cvt_pk_bf16_f32 v130, v168, v172\n"
      "v_cvt_pk_bf16_f32 v134, v169, v173\n"
      "v_cvt_pk_bf16_f32 v138, v170, v174\n"
      "v_cvt_pk_bf16_f32 v142, v171, v175\n"
      "s_waitcnt vmcnt(0)\n"
      "v_fma_mix_f32 v176, v56, v96, v100 op_sel:[0,0,0] op_sel_hi:[1,0,0]\n"
      "v_fma_mix_f32 v177, v56, v97, v101 op_sel:[1,0,0] op_sel_hi:[1,0,0]\n"
      "v_fma_mix_f32 v178, v57, v98, v102 op_sel:[0,0,0] op_sel_hi:[1,0,0]\n"
      "v_fma_mix_f32 v179, v57, v99, v103 op_sel:[1,0,0] op_sel_hi:[1,0,0]\n"
      "v_fma_mix_f32 v180, v60, v96, v100 op_sel:[0,0,0] op_sel_hi:[1,0,0]\n"
      "v_fma_mix_f32 v181, v60, v97, v101 op_sel:[1,0,0] op_sel_hi:[1,0,0]\n"
      "v_fma_mix_f32 v182, v61, v98, v102 op_sel:[0,0,0] op_sel_hi:[1,0,0]\n"
      "v_fma_mix_f32 v183, v61, v99, v103 op_sel:[1,0,0] op_sel_hi:[1,0,0]\n"
      "v_mul_f32 v176, v110, v176\n"
      "v_mul_f32 v177, v110, v177\n"
      "v_mul_f32 v178, v110, v178\n"
      "v_mul_f32 v179, v110, v179\n"
      "v_mul_f32 v180, v111, v180\n"
      "v_mul_f32 v181, v111, v181\n"
      "v_mul_f32 v182, v111, v182\n"
      "v_mul_f32 v183, v111, v183\n"
      "v_cvt_pk_bf16_f32 v131, v176, v180\n"
      "v_cvt_pk_bf16_f32 v135, v177, v181\n"
      "v_cvt_pk_bf16_f32 v139, v178, v182\n"
      "v_cvt_pk_bf16_f32 v143, v179, v183\n"
      "ds_write_b128 v158, v[128:131] offset:0\n"
      "ds_write_b128 v158, v[132:135] offset:2048\n"
      "ds_write_b128 v158, v[136:139] offset:4096\n"
      "ds_write_b128 v158, v[140:143] offset:6144\n"
      "buffer_load_format_d16_xyzw v[64:65], v144, s[24:27], s43 offen\n"
      "buffer_load_format_d16_xyzw v[68:69], v145, s[24:27], s43 offen\n"
      "buffer_load_format_d16_xyzw v[72:73], v146, s[24:27], s43 offen\n"
      "buffer_load_format_d16_xyzw v[76:77], v147, s[24:27], s43 offen\n"
      "buffer_load_format_d16_xyzw v[80:81], v148, s[24:27], s43 offen\n"
      "buffer_load_format_d16_xyzw v[84:85], v149, s[24:27], s43 offen\n"
      "buffer_load_format_d16_xyzw v[88:89], v150, s[24:27], s43 offen\n"
      "buffer_load_format_d16_xyzw v[92:93], v151, s[24:27], s43 offen\n"
      "s_mov_b32 s42, s41\n"
      "s_lshr_b64 s[48:49], s[48:49], 4\n"
      "s_and_b32 s41, s48, 15\n"
      "s_mul_i32 s43, s41, 917504\n"
      "s_waitcnt lgkmcnt(0)\n"
      "s_barrier\n"
      "v_lshl_add_u32 v156, s42, 9, v159\n"
      "v_lshl_add_u32 v157, s42, 8, v160\n"
      "ds_read_b128 v[112:115], v156\n"
      "ds_read_b128 v[116:119], v156 offset:8192\n"
      "ds_read_b128 v[120:123], v157\n"
      "ds_read_b128 v[124:127], v157 offset:16\n"
      "v_lshl_add_u32 v156, s41, 9, v159\n"
      "v_lshl_add_u32 v157, s41, 8, v160\n"
      "ds_read_b128 v[96:99], v156\n"
      "ds_read_b128 v[100:103], v156 offset:8192\n"
      "ds_read_b128 v[104:107], v157\n"
      "ds_read_b128 v[108:111], v157 offset:16\n"
      "buffer_load_format_d16_xyzw v[32:33], v144, s[24:27], s43 offen\n"
      "s_waitcnt lgkmcnt(0)\n"
      "s_waitcnt vmcnt(7)\n"
      "v_fma_mix_f32 v168, v64, v112, v116 op_sel:[0,0,0] op_sel_hi:[1,0,0]\n"
      "v_fma_mix_f32 v169, v64, v113, v117 op_sel:[1,0,0] op_sel_hi:[1,0,0]\n"
      "v_fma_mix_f32 v170, v65, v114, v118 op_sel:[0,0,0] op_sel_hi:[1,0,0]\n"
      "v_fma_mix_f32 v171, v65, v115, v119 op_sel:[1,0,0] op_sel_hi:[1,0,0]\n"
      "v_fma_mix_f32 v172, v68, v112, v116 op_sel:[0,0,0] op_sel_hi:[1,0,0]\n"
      "v_fma_mix_f32 v173, v68, v113, v117 op_sel:[1,0,0] op_sel_hi:[1,0,0]\n"
      "v_fma_mix_f32 v174, v69, v114, v118 op_sel:[0,0,0] op_sel_hi:[1,0,0]\n"
      "v_fma_mix_f32 v175, v69, v115, v119 op_sel:[1,0,0] op_sel_hi:[1,0,0]\n"
      "v_mul_f32 v168, v120, v168\n"
      "v_mul_f32 v169, v120, v169\n"
      "buffer_load_format_d16_xyzw v[36:37], v145, s[24:27], s43 offen\n"
      "v_mul_f32 v170, v120, v170\n"
      "v_mul_f32 v171, v120, v171\n"
      "v_mul_f32 v172, v121, v172\n"
      "v_mul_f32 v173, v121, v173\n"
      "v_mul_f32 v174, v121, v174\n"
      "v_mul_f32 v175, v121, v175\n"
      "v_cvt_pk_bf16_f32 v128, v168, v172\n"
      "v_cvt_pk_bf16_f32 v132, v169, v173\n"
      "v_cvt_pk_bf16_f32 v136, v170, v174\n"
      "v_cvt_pk_bf16_f32 v140, v171, v175\n"
      "buffer_load_format_d16_xyzw v[40:41], v146, s[24:27], s43 offen\n"
      "s_waitcnt vmcnt(7)\n"
      "v_fma_mix_f32 v176, v72, v112, v116 op_sel:[0,0,0] op_sel_hi:[1,0,0]\n"
      "v_fma_mix_f32 v177, v72, v113, v117 op_sel:[1,0,0] op_sel_hi:[1,0,0]\n"
      "v_fma_mix_f32 v178, v73, v114, v118 op_sel:[0,0,0] op_sel_hi:[1,0,0]\n"
      "v_fma_mix_f32 v179, v73, v115, v119 op_sel:[1,0,0] op_sel_hi:[1,0,0]\n"
      "v_fma_mix_f32 v180, v76, v112, v116 op_sel:[0,0,0] op_sel_hi:[1,0,0]\n"
      "v_fma_mix_f32 v181, v76, v113, v117 op_sel:[1,0,0] op_sel_hi:[1,0,0]\n"
      "v_fma_mix_f32 v182, v77, v114, v118 op_sel:[0,0,0] op_sel_hi:[1,0,0]\n"
      "v_fma_mix_f32 v183, v77, v115, v119 op_sel:[1,0,0] op_sel_hi:[1,0,0]\n"
      "v_mul_f32 v176, v122, v176\n"
      "v_mul_f32 v177, v122, v177\n"
      "buffer_load_format_d16_xyzw v[44:45], v147, s[24:27], s43 offen\n"
      "v_mul_f32 v178, v122, v178\n"
      "v_mul_f32 v179, v122, v179\n"
      "v_mul_f32 v180, v123, v180\n"
      "v_mul_f32 v181, v123, v181\n"
      "v_mul_f32 v182, v123, v182\n"
      "v_mul_f32 v183, v123, v183\n"
      "v_cvt_pk_bf16_f32 v129, v176, v180\n"
      "v_cvt_pk_bf16_f32 v133, v177, v181\n"
      "v_cvt_pk_bf16_f32 v137, v178, v182\n"
      "v_cvt_pk_bf16_f32 v141, v179, v183\n"
      "buffer_load_format_d16_xyzw v[48:49], v148, s[24:27], s43 offen\n"
      "s_waitcnt vmcnt(7)\n"
      "v_fma_mix_f32 v168, v80, v112, v116 op_sel:[0,0,0] op_sel_hi:[1,0,0]\n"
      "v_fma_mix_f32 v169, v80, v113, v117 op_sel:[1,0,0] op_sel_hi:[1,0,0]\n"
      "v_fma_mix_f32 v170, v81, v114, v118 op_sel:[0,0,0] op_sel_hi:[1,0,0]\n"
      "v_fma_mix_f32 v171, v81, v115, v119 op_sel:[1,0,0] op_sel_hi:[1,0,0]\n"
      "v_fma_mix_f32 v172, v84, v112, v116 op_sel:[0,0,0] op_sel_hi:[1,0,0]\n"
      "v_fma_mix_f32 v173, v84, v113, v117 op_sel:[1,0,0] op_sel_hi:[1,0,0]\n"
      "v_fma_mix_f32 v174, v85, v114, v118 op_sel:[0,0,0] op_sel_hi:[1,0,0]\n"
      "v_fma_mix_f32 v175, v85, v115, v119 op_sel:[1,0,0] op_sel_hi:[1,0,0]\n"
      "v_mul_f32 v168, v124, v168\n"
      "v_mul_f32 v169, v124, v169\n"
      "buffer_load_format_d16_xyzw v[52:53], v149, s[24:27], s43 offen\n"
      "v_mul_f32 v170, v124, v170\n"
      "v_mul_f32 v171, v124, v171\n"
      "v_mul_f32 v172, v125, v172\n"
      "v_mul_f32 v173, v125, v173\n"
      "v_mul_f32 v174, v125, v174\n"
      "v_mul_f32 v175, v125, v175\n"
      "v_cvt_pk_bf16_f32 v130, v168, v172\n"
      "v_cvt_pk_bf16_f32 v134, v169, v173\n"
      "v_cvt_pk_bf16_f32 v138, v170, v174\n"
      "v_cvt_pk_bf16_f32 v142, v171, v175\n"
      "buffer_load_format_d16_xyzw v[56:57], v150, s[24:27], s43 offen\n"
      "s_waitcnt vmcnt(7)\n"
      "v_fma_mix_f32 v176, v88, v112, v116 op_sel:[0,0,0] op_sel_hi:[1,0,0]\n"
      "v_fma_mix_f32 v177, v88, v113, v117 op_sel:[1,0,0] op_sel_hi:[1,0,0]\n"
      "v_fma_mix_f32 v178, v89, v114, v118 op_sel:[0,0,0] op_sel_hi:[1,0,0]\n"
      "v_fma_mix_f32 v179, v89, v115, v119 op_sel:[1,0,0] op_sel_hi:[1,0,0]\n"
      "v_fma_mix_f32 v180, v92, v112, v116 op_sel:[0,0,0] op_sel_hi:[1,0,0]\n"
      "v_fma_mix_f32 v181, v92, v113, v117 op_sel:[1,0,0] op_sel_hi:[1,0,0]\n"
      "v_fma_mix_f32 v182, v93, v114, v118 op_sel:[0,0,0] op_sel_hi:[1,0,0]\n"
      "v_fma_mix_f32 v183, v93, v115, v119 op_sel:[1,0,0] op_sel_hi:[1,0,0]\n"
      "v_mul_f32 v176, v126, v176\n"
      "v_mul_f32 v177, v126, v177\n"
      "buffer_load_format_d16_xyzw v[60:61], v151, s[24:27], s43 offen\n"
      "v_mul_f32 v178, v126, v178\n"
      "v_mul_f32 v179, v126, v179\n"
      "v_mul_f32 v180, v127, v180\n"
      "v_mul_f32 v181, v127, v181\n"
      "v_mul_f32 v182, v127, v182\n"
      "v_mul_f32 v183, v127, v183\n"
      "v_cvt_pk_bf16_f32 v131, v176, v180\n"
      "v_cvt_pk_bf16_f32 v135, v177, v181\n"
      "v_cvt_pk_bf16_f32 v139, v178, v182\n"
      "v_cvt_pk_bf16_f32 v143, v179, v183\n"
      "s_lshr_b64 s[48:49], s[48:49], 4\n"
      "s_and_b32 s41, s48, 15\n"
      "s_mul_i32 s43, s41, 917504\n"
      "s_branch L_pent_%=\n"
      "L_cons_%=:\n"
      "s_mov_b32 s20, s8\n"
      "s_and_b32 s21, s9, 0xffff\n"
      "s_mov_b32 s22, 4194304\n"
      "s_mov_b32 s23, 0x20000\n"
      "s_lshl_b32 s40, s5, 19\n"
      "v_lshrrev_b32 v42, 4, v1\n"
      "v_xor_b32 v42, v42, v1\n"
      "v_and_b32 v42, 7, v42\n"
      "v_lshrrev_b32 v43, 3, v1\n"
      "v_lshlrev_b32 v43, 11, v43\n"
      "v_lshl_add_u32 v42, v42, 4, v43\n"
      "v_add_u32 v16, s40, v42\n"
      "v_xor_b32 v17, 64, v16\n"
      "v_add_u32 v18, 30720, v16\n"
      "v_add_u32 v19, 46080, v17\n"
      "v_add_u32 v17, 15360, v17\n"
      "s_lshr_b32 s40, s35, 10\n"
      "s_lshr_b32 s44, 0x1c1c1814, s40\n"
      "s_lshr_b32 s44, s49, s44\n"
      "s_and_b32 s44, s44, 15\n"
      "s_lshl_b32 s44, s44, 7\n"
      "s_lshr_b32 s45, 0x1000000, s40\n"
      "s_and_b32 s45, s45, 3\n"
      "s_lshl_b32 s45, s45, 16\n"
      "s_add_u32 s53, s44, s45\n"
      "s_lshr_b32 s40, s35, 10\n"
      "s_lshr_b32 s44, 0x18141418, s40\n"
      "s_lshr_b32 s44, s49, s44\n"
      "s_and_b32 s44, s44, 15\n"
      "s_lshl_b32 s44, s44, 7\n"
      "s_lshr_b32 s45, 0x2020101, s40\n"
      "s_and_b32 s45, s45, 3\n"
      "s_lshl_b32 s45, s45, 16\n"
      "s_add_u32 s54, s44, s45\n"
      "s_mov_b32 s47, 0x10000\n"
      "s_and_b32 s42, s48, 15\n"
      "s_lshr_b64 s[48:49], s[48:49], 4\n"
      "s_lshl_b32 s42, s42, 7\n"
      "s_lshl4_add_u32 s42, s35, s42\n"
      "s_add_u32 s43, s35, 0\n"
      "s_mov_b32 m0, s43\n"
      "s_nop 0\n"
      "v_mov_b64 v[128:129], 0\n"
      "v_mov_b64 v[130:131], 0\n"
      "v_mov_b64 v[132:133], 0\n"
      "v_mov_b64 v[134:135], 0\n"
      "v_mov_b64 v[136:137], 0\n"
      "v_mov_b64 v[138:139], 0\n"
      "v_mov_b64 v[140:141], 0\n"
      "v_mov_b64 v[142:143], 0\n"
      "v_mov_b64 v[144:145], 0\n"
      "v_mov_b64 v[146:147], 0\n"
      "v_mov_b64 v[148:149], 0\n"
      "v_mov_b64 v[150:151], 0\n"
      "v_mov_b64 v[152:153], 0\n"
      "v_mov_b64 v[154:155], 0\n"
      "v_mov_b64 v[156:157], 0\n"
      "v_mov_b64 v[158:159], 0\n"
      "v_mov_b64 v[160:161], 0\n"
      "v_mov_b64 v[162:163], 0\n"
      "v_mov_b64 v[164:165], 0\n"
      "v_mov_b64 v[166:167], 0\n"
      "v_mov_b64 v[168:169], 0\n"
      "v_mov_b64 v[170:171], 0\n"
      "v_mov_b64 v[172:173], 0\n"
      "v_mov_b64 v[174:175], 0\n"
      "v_mov_b64 v[176:177], 0\n"
      "v_mov_b64 v[178:179], 0\n"
      "v_mov_b64 v[180:181], 0\n"
      "v_mov_b64 v[182:183], 0\n"
      "v_mov_b64 v[184:185], 0\n"
      "v_mov_b64 v[186:187], 0\n"
      "v_mov_b64 v[188:189], 0\n"
      "v_mov_b64 v[190:191], 0\n"
      "v_mov_b64 v[192:193], 0\n"
      "v_mov_b64 v[194:195], 0\n"
      "v_mov_b64 v[196:197], 0\n"
      "v_mov_b64 v[198:199], 0\n"
      "v_mov_b64 v[200:201], 0\n"
      "v_mov_b64 v[202:203], 0\n"
      "v_mov_b64 v[204:205], 0\n"
      "v_mov_b64 v[206:207], 0\n"
      "v_mov_b64 v[208:209], 0\n"
      "v_mov_b64 v[210:211], 0\n"
      "v_mov_b64 v[212:213], 0\n"
      "v_mov_b64 v[214:215], 0\n"
      "v_mov_b64 v[216:217], 0\n"
      "v_mov_b64 v[218:219], 0\n"
      "v_mov_b64 v[220:221], 0\n"
      "v_mov_b64 v[222:223], 0\n"
      "v_mov_b64 v[224:225], 0\n"
      "v_mov_b64 v[226:227], 0\n"
      "v_mov_b64 v[228:229], 0\n"
      "v_mov_b64 v[230:231], 0\n"
      "v_mov_b64 v[232:233], 0\n"
      "v_mov_b64 v[234:235], 0\n"
      "v_mov_b64 v[236:237], 0\n"
      "v_mov_b64 v[238:239], 0\n"
      "v_mov_b64 v[240:241], 0\n"
      "v_mov_b64 v[242:243], 0\n"
      "v_mov_b64 v[244:245], 0\n"
      "v_mov_b64 v[246:247], 0\n"
      "v_mov_b64 v[248:249], 0\n"
      "v_mov_b64 v[250:251], 0\n"
      "v_mov_b64 v[252:253], 0\n"
      "v_mov_b64 v[254:255], 0\n"
      "buffer_load_dwordx4 v16, s[20:23], s42 offen offset:0 lds\n"
      "buffer_load_dwordx4 v17, s[20:23], s42 offen offset:1024 lds\n"
      "buffer_load_dwordx4 v18, s[20:23], s42 offen offset:2048 lds\n"
      "buffer_load_dwordx4 v19, s[20:23], s42 offen offset:3072 lds\n"
      "s_add_u32 m0, s43, 4096\n"
      "s_add_u32 s42, s42, s47\n"
      "buffer_load_dwordx4 v16, s[20:23], s42 offen offset:0 lds\n"
      "buffer_load_dwordx4 v17, s[20:23], s42 offen offset:1024 lds\n"
      "buffer_load_dwordx4 v18, s[20:23], s42 offen offset:2048 lds\n"
      "buffer_load_dwordx4 v19, s[20:23], s42 offen offset:3072 lds\n"
      "s_branch L_stub_%=\n"
      "L_back_%=:\n"
      "s_and_b32 s42, s48, 15\n"
      "s_lshr_b64 s[48:49], s[48:49], 4\n"
      "s_lshl_b32 s42, s42, 7\n"
      "s_lshl4_add_u32 s42, s35, s42\n"
      "s_add_u32 s43, s35, 32768\n"
      "s_mov_b32 m0, s43\n"
      "v_lshlrev_b32 v6, 9, v6\n"
      "v_lshl_add_u32 v6, v28, 5, v6\n"
      "v_add_u32 v6, 135168, v6\n"
      "v_lshlrev_b32 v5, 4, v5\n"
      "v_add_u32 v5, 131072, v5\n"
      "s_mov_b32 s28, s18\n"
      "s_and_b32 s29, s19, 0xffff\n"
      "s_mov_b32 s30, 14680064\n"
      "s_mov_b32 s31, 0x20000\n"
      "v_and_b32 v28, 15, v1\n"
      "v_lshrrev_b32 v29, 4, v1\n"
      "v_bfe_u32 v30, v28, 1, 3\n"
      "v_xor_b32 v30, v30, v29\n"
      "v_lshlrev_b32 v30, 4, v30\n"
      "v_lshl_add_u32 v30, v28, 7, v30\n"
      "s_bfe_u32 s40, s4, 0x10001\n"
      "s_and_b32 s44, s4, 1\n"
      "s_lshl_b32 s45, s40, 14\n"
      "v_add_u32 v8, s45, v30\n"
      "v_and_b32 v30, 7, v28\n"
      "v_xor_b32 v30, v30, v29\n"
      "v_lshlrev_b32 v30, 4, v30\n"
      "v_lshl_add_u32 v30, v28, 7, v30\n"
      "s_lshl_b32 s45, s44, 13\n"
      "s_add_u32 s45, s45, 98304\n"
      "v_add_u32 v12, s45, v30\n"
      "v_xor_b32 v9, 64, v8\n"
      "v_xor_b32 v13, 64, v12\n"
      "s_mul_i32 s45, s40, 12288\n"
      "v_subrev_u32 v20, s45, v8\n"
      "v_add_u32 v22, 0x10000, v20\n"
      "v_subrev_u32 v21, s45, v9\n"
      "v_add_u32 v23, 0x10000, v21\n"
      "s_lshl1_add_u32 s40, s5, s40\n"
      "s_mul_i32 s40, s40, 917504\n"
      "s_lshl1_add_u32 s44, s34, s44\n"
      "s_lshl_b32 s44, s44, 7\n"
      "s_add_u32 s40, s40, s44\n"
      "v_lshl_add_u32 v2, v28, 3, s40\n"
      "s_movk_i32 s40, 28672\n"
      "v_mad_u32_u24 v2, v29, s40, v2\n"
      "v_add_u32 v3, 7168, v2\n"
      "v_add_u32 v4, 14336, v2\n"
      "v_add_u32 v10, 21504, v2\n"
      "s_mov_b32 s46, 0\n"
      "s_mov_b32 s41, 0\n"
      "s_waitcnt vmcnt(8)\n"
      "v_lshlrev_b32 v48, 16, v32\n"
      "v_and_b32 v49, 0xffff0000, v32\n"
      "v_lshlrev_b32 v50, 16, v33\n"
      "v_and_b32 v51, 0xffff0000, v33\n"
      "v_lshlrev_b32 v52, 16, v34\n"
      "v_and_b32 v53, 0xffff0000, v34\n"
      "v_lshlrev_b32 v54, 16, v35\n"
      "v_and_b32 v55, 0xffff0000, v35\n"
      "v_lshlrev_b32 v56, 16, v36\n"
      "v_and_b32 v57, 0xffff0000, v36\n"
      "v_lshlrev_b32 v58, 16, v37\n"
      "v_and_b32 v59, 0xffff0000, v37\n"
      "v_lshlrev_b32 v60, 16, v38\n"
      "v_and_b32 v61, 0xffff0000, v38\n"
      "v_lshlrev_b32 v62, 16, v39\n"
      "v_and_b32 v63, 0xffff0000, v39\n"
      "v_lshlrev_b32 v64, 16, v40\n"
      "v_and_b32 v65, 0xffff0000, v40\n"
      "v_lshlrev_b32 v66, 16, v41\n"
      "v_and_b32 v67, 0xffff0000, v41\n"
      "v_mul_f32 v56, v56, v48\n"
      "v_mul_f32 v57, v57, v49\n"
      "v_mul_f32 v58, v58, v50\n"
      "v_mul_f32 v59, v59, v51\n"
      "v_mul_f32 v60, v60, v52\n"
      "v_mul_f32 v61, v61, v53\n"
      "v_mul_f32 v62, v62, v54\n"
      "v_mul_f32 v63, v63, v55\n"
      "v_mul_f32 v56, s38, v56\n"
      "v_mul_f32 v57, s38, v57\n"
      "v_mul_f32 v58, s38, v58\n"
      "v_mul_f32 v59, s38, v59\n"
      "v_mul_f32 v60, s38, v60\n"
      "v_mul_f32 v61, s38, v61\n"
      "v_mul_f32 v62, s38, v62\n"
      "v_mul_f32 v63, s38, v63\n"
      "v_mul_f32 v48, s37, v48\n"
      "v_mul_f32 v49, s37, v49\n"
      "v_mul_f32 v50, s37, v50\n"
      "v_mul_f32 v51, s37, v51\n"
      "v_mul_f32 v52, s37, v52\n"
      "v_mul_f32 v53, s37, v53\n"
      "v_mul_f32 v54, s37, v54\n"
      "v_mul_f32 v55, s37, v55\n"
      "ds_write_b128 v6, v[48:51]\n"
      "ds_write_b128 v6, v[52:55] offset:16\n"
      "ds_write_b128 v6, v[56:59] offset:8192\n"
      "ds_write_b128 v6, v[60:63] offset:8208\n"
      "ds_write_b128 v5, v[64:67]\n"
      "s_setprio 1\n"
      "s_waitcnt vmcnt(0) lgkmcnt(0)\n"
      "s_barrier\n"
      "ds_read_b128 v[32:35], v8 offset:0\n"
      "ds_read_b128 v[64:67], v12 offset:0\n"
      "ds_read_b128 v[36:39], v9 offset:0\n"
      "ds_read_b128 v[68:71], v13 offset:0\n"
      "ds_read_b128 v[72:75], v12 offset:2048\n"
      "ds_read_b128 v[76:79], v13 offset:2048\n"
      "ds_read_b128 v[80:83], v12 offset:4096\n"
      "ds_read_b128 v[84:87], v13 offset:4096\n"
      "buffer_load_dwordx4 v16, s[20:23], s42 offen offset:0 lds\n"
      "buffer_load_dwordx4 v17, s[20:23], s42 offen offset:1024 lds\n"
      "buffer_load_dwordx4 v18, s[20:23], s42 offen offset:2048 lds\n"
      "buffer_load_dwordx4 v19, s[20:23], s42 offen offset:3072 lds\n"
      "s_add_u32 m0, s43, 4096\n"
      "s_add_u32 s42, s42, s47\n"
      "buffer_load_dwordx4 v16, s[20:23], s42 offen offset:0 lds\n"
      "s_branch L_cent_%=\n"
      ".p2alignl 12, 0xBF800000\n"
      "L_stubp_%=:\n"
      "s_branch L_backp_%=\n"
      "L_ploop_%=:\n"
      "s_barrier\n"
      "L_pent_%=:\n"
      "ds_write_b128 v158, v[128:131] offset:16384\n"
      "ds_write_b128 v158, v[132:135] offset:18432\n"
      "ds_write_b128 v158, v[136:139] offset:20480\n"
      "ds_write_b128 v158, v[140:143] offset:22528\n"
      "s_waitcnt vmcnt(6)\n"
      "v_fma_mix_f32 v168, v32, v96, v100 op_sel:[0,0,0] op_sel_hi:[1,0,0]\n"
      "v_fma_mix_f32 v169, v32, v97, v101 op_sel:[1,0,0] op_sel_hi:[1,0,0]\n"
      "v_fma_mix_f32 v170, v33, v98, v102 op_sel:[0,0,0] op_sel_hi:[1,0,0]\n"
      "v_fma_mix_f32 v171, v33, v99, v103 op_sel:[1,0,0] op_sel_hi:[1,0,0]\n"
      "v_fma_mix_f32 v172, v36, v96, v100 op_sel:[0,0,0] op_sel_hi:[1,0,0]\n"
      "v_fma_mix_f32 v173, v36, v97, v101 op_sel:[1,0,0] op_sel_hi:[1,0,0]\n"
      "v_fma_mix_f32 v174, v37, v98, v102 op_sel:[0,0,0] op_sel_hi:[1,0,0]\n"
      "v_fma_mix_f32 v175, v37, v99, v103 op_sel:[1,0,0] op_sel_hi:[1,0,0]\n"
      "v_mul_f32 v168, v104, v168\n"
      "v_mul_f32 v169, v104, v169\n"
      "v_mul_f32 v170, v104, v170\n"
      "v_mul_f32 v171, v104, v171\n"
      "v_mul_f32 v172, v105, v172\n"
      "v_mul_f32 v173, v105, v173\n"
      "v_mul_f32 v174, v105, v174\n"
      "v_mul_f32 v175, v105, v175\n"
      "v_cvt_pk_bf16_f32 v128, v168, v172\n"
      "v_cvt_pk_bf16_f32 v132, v169, v173\n"
      "v_cvt_pk_bf16_f32 v136, v170, v174\n"
      "v_cvt_pk_bf16_f32 v140, v171, v175\n"
      "s_waitcnt vmcnt(4)\n"
      "v_fma_mix_f32 v176, v40, v96, v100 op_sel:[0,0,0] op_sel_hi:[1,0,0]\n"
      "v_fma_mix_f32 v177, v40, v97, v101 op_sel:[1,0,0] op_sel_hi:[1,0,0]\n"
      "v_fma_mix_f32 v178, v41, v98, v102 op_sel:[0,0,0] op_sel_hi:[1,0,0]\n"
      "v_fma_mix_f32 v179, v41, v99, v103 op_sel:[1,0,0] op_sel_hi:[1,0,0]\n"
      "v_fma_mix_f32 v180, v44, v96, v100 op_sel:[0,0,0] op_sel_hi:[1,0,0]\n"
      "v_fma_mix_f32 v181, v44, v97, v101 op_sel:[1,0,0] op_sel_hi:[1,0,0]\n"
      "v_fma_mix_f32 v182, v45, v98, v102 op_sel:[0,0,0] op_sel_hi:[1,0,0]\n"
      "v_fma_mix_f32 v183, v45, v99, v103 op_sel:[1,0,0] op_sel_hi:[1,0,0]\n"
      "v_mul_f32 v176, v106, v176\n"
      "v_mul_f32 v177, v106, v177\n"
      "v_mul_f32 v178, v106, v178\n"
      "v_mul_f32 v179, v106, v179\n"
      "v_mul_f32 v180, v107, v180\n"
      "v_mul_f32 v181, v107, v181\n"
      "v_mul_f32 v182, v107, v182\n"
      "v_mul_f32 v183, v107, v183\n"
      "v_cvt_pk_bf16_f32 v129, v176, v180\n"
      "v_cvt_pk_bf16_f32 v133, v177, v181\n"
      "v_cvt_pk_bf16_f32 v137, v178, v182\n"
      "v_cvt_pk_bf16_f32 v141, v179, v183\n"
      "s_waitcnt vmcnt(2)\n"
      "v_fma_mix_f32 v168, v48, v96, v100 op_sel:[0,0,0] op_sel_hi:[1,0,0]\n"
      "v_fma_mix_f32 v169, v48, v97, v101 op_sel:[1,0,0] op_sel_hi:[1,0,0]\n"
      "v_fma_mix_f32 v170, v49, v98, v102 op_sel:[0,0,0] op_sel_hi:[1,0,0]\n"
      "v_fma_mix_f32 v171, v49, v99, v103 op_sel:[1,0,0] op_sel_hi:[1,0,0]\n"
      "v_fma_mix_f32 v172, v52, v96, v100 op_sel:[0,0,0] op_sel_hi:[1,0,0]\n"
      "v_fma_mix_f32 v173, v52, v97, v101 op_sel:[1,0,0] op_sel_hi:[1,0,0]\n"
      "v_fma_mix_f32 v174, v53, v98, v102 op_sel:[0,0,0] op_sel_hi:[1,0,0]\n"
      "v_fma_mix_f32 v175, v53, v99, v103 op_sel:[1,0,0] op_sel_hi:[1,0,0]\n"
      "v_mul_f32 v168, v108, v168\n"
      "v_mul_f32 v169, v108, v169\n"
      "v_mul_f32 v170, v108, v170\n"
      "v_mul_f32 v171, v108, v171\n"
      "v_lshl_add_u32 v156, s41, 9, v159\n"
      "v_lshl_add_u32 v157, s41, 8, v160\n"
      "buffer_load_format_d16_xyzw v[64:65], v144, s[24:27], s43 offen\n"
      "v_mul_f32 v172, v109, v172\n"
      "v_mul_f32 v173, v109, v173\n"
      "v_mul_f32 v174, v109, v174\n"
      "v_mul_f32 v175, v109, v175\n"
      "buffer_load_format_d16_xyzw v[68:69], v145, s[24:27], s43 offen\n"
      "v_cvt_pk_bf16_f32 v130, v168, v172\n"
      "v_cvt_pk_bf16_f32 v134, v169, v173\n"
      "v_cvt_pk_bf16_f32 v138, v170, v174\n"
      "v_cvt_pk_bf16_f32 v142, v171, v175\n"
      "ds_read_b128 v[112:115], v156\n"
      "buffer_load_format_d16_xyzw v[72:73], v146, s[24:27], s43 offen\n"
      "s_waitcnt vmcnt(3)\n"
      "v_fma_mix_f32 v176, v56, v96, v100 op_sel:[0,0,0] op_sel_hi:[1,0,0]\n"
      "v_fma_mix_f32 v177, v56, v97, v101 op_sel:[1,0,0] op_sel_hi:[1,0,0]\n"
      "v_fma_mix_f32 v178, v57, v98, v102 op_sel:[0,0,0] op_sel_hi:[1,0,0]\n"
      "v_fma_mix_f32 v179, v57, v99, v103 op_sel:[1,0,0] op_sel_hi:[1,0,0]\n"
      "ds_read_b128 v[116:119], v156 offset:8192\n"
      "buffer_load_format_d16_xyzw v[76:77], v147, s[24:27], s43 offen\n"
      "v_fma_mix_f32 v180, v60, v96, v100 op_sel:[0,0,0] op_sel_hi:[1,0,0]\n"
      "v_fma_mix_f32 v181, v60, v97, v101 op_sel:[1,0,0] op_sel_hi:[1,0,0]\n"
      "v_fma_mix_f32 v182, v61, v98, v102 op_sel:[0,0,0] op_sel_hi:[1,0,0]\n"
      "v_fma_mix_f32 v183, v61, v99, v103 op_sel:[1,0,0] op_sel_hi:[1,0,0]\n"
      "ds_read_b128 v[120:123], v157\n"
      "buffer_load_format_d16_xyzw v[80:81], v148, s[24:27], s43 offen\n"
      "v_mul_f32 v176, v110, v176\n"
      "v_mul_f32 v177, v110, v177\n"
      "v_mul_f32 v178, v110, v178\n"
      "v_mul_f32 v179, v110, v179\n"
      "ds_read_b128 v[124:127], v157 offset:16\n"
      "s_lshr_b64 s[48:49], s[48:49], 4\n"
      "buffer_load_format_d16_xyzw v[84:85], v149, s[24:27], s43 offen\n"
      "v_mul_f32 v180, v111, v180\n"
      "v_mul_f32 v181, v111, v181\n"
      "v_mul_f32 v182, v111, v182\n"
      "v_mul_f32 v183, v111, v183\n"
      "s_and_b32 s41, s48, 15\n"
      "s_mul_i32 s47, s41, 917504\n"
      "buffer_load_format_d16_xyzw v[88:89], v150, s[24:27], s43 offen\n"
      "v_cvt_pk_bf16_f32 v131, v176, v180\n"
      "v_cvt_pk_bf16_f32 v135, v177, v181\n"
      "v_cvt_pk_bf16_f32 v139, v178, v182\n"
      "v_cvt_pk_bf16_f32 v143, v179, v183\n"
      "s_cmp_eq_u32 s46, 6\n"
      "buffer_load_format_d16_xyzw v[92:93], v151, s[24:27], s43 offen\n"
      "s_waitcnt lgkmcnt(0)\n"
      "s_cbranch_scc1 L_pend_%=\n"
      "s_barrier\n"
      "ds_write_b128 v158, v[128:131] offset:0\n"
      "ds_write_b128 v158, v[132:135] offset:2048\n"
      "ds_write_b128 v158, v[136:139] offset:4096\n"
      "ds_write_b128 v158, v[140:143] offset:6144\n"
      "s_waitcnt vmcnt(6)\n"
      "v_fma_mix_f32 v168, v64, v112, v116 op_sel:[0,0,0] op_sel_hi:[1,0,0]\n"
      "v_fma_mix_f32 v169, v64, v113, v117 op_sel:[1,0,0] op_sel_hi:[1,0,0]\n"
      "v_fma_mix_f32 v170, v65, v114, v118 op_sel:[0,0,0] op_sel_hi:[1,0,0]\n"
      "v_fma_mix_f32 v171, v65, v115, v119 op_sel:[1,0,0] op_sel_hi:[1,0,0]\n"
      "v_fma_mix_f32 v172, v68, v112, v116 op_sel:[0,0,0] op_sel_hi:[1,0,0]\n"
      "v_fma_mix_f32 v173, v68, v113, v117 op_sel:[1,0,0] op_sel_hi:[1,0,0]\n"
      "v_fma_mix_f32 v174, v69, v114, v118 op_sel:[0,0,0] op_sel_hi:[1,0,0]\n"
      "v_fma_mix_f32 v175, v69, v115, v119 op_sel:[1,0,0] op_sel_hi:[1,0,0]\n"
      "v_mul_f32 v168, v120, v168\n"
      "v_mul_f32 v169, v120, v169\n"
      "v_mul_f32 v170, v120, v170\n"
      "v_mul_f32 v171, v120, v171\n"
      "v_mul_f32 v172, v121, v172\n"
      "v_mul_f32 v173, v121, v173\n"
      "v_mul_f32 v174, v121, v174\n"
      "v_mul_f32 v175, v121, v175\n"
      "v_cvt_pk_bf16_f32 v128, v168, v172\n"
      "v_cvt_pk_bf16_f32 v132, v169, v173\n"
      "v_cvt_pk_bf16_f32 v136, v170, v174\n"
      "v_cvt_pk_bf16_f32 v140, v171, v175\n"
      "s_waitcnt vmcnt(4)\n"
      "v_fma_mix_f32 v176, v72, v112, v116 op_sel:[0,0,0] op_sel_hi:[1,0,0]\n"
      "v_fma_mix_f32 v177, v72, v113, v117 op_sel:[1,0,0] op_sel_hi:[1,0,0]\n"
      "v_fma_mix_f32 v178, v73, v114, v118 op_sel:[0,0,0] op_sel_hi:[1,0,0]\n"
      "v_fma_mix_f32 v179, v73, v115, v119 op_sel:[1,0,0] op_sel_hi:[1,0,0]\n"
      "v_fma_mix_f32 v180, v76, v112, v116 op_sel:[0,0,0] op_sel_hi:[1,0,0]\n"
      "v_fma_mix_f32 v181, v76, v113, v117 op_sel:[1,0,0] op_sel_hi:[1,0,0]\n"
      "v_fma_mix_f32 v182, v77, v114, v118 op_sel:[0,0,0] op_sel_hi:[1,0,0]\n"
      "v_fma_mix_f32 v183, v77, v115, v119 op_sel:[1,0,0] op_sel_hi:[1,0,0]\n"
      "v_mul_f32 v176, v122, v176\n"
      "v_mul_f32 v177, v122, v177\n"
      "v_mul_f32 v178, v122, v178\n"
      "v_mul_f32 v179, v122, v179\n"
      "v_mul_f32 v180, v123, v180\n"
      "v_mul_f32 v181, v123, v181\n"
      "v_mul_f32 v182, v123, v182\n"
      "v_mul_f32 v183, v123, v183\n"
      "v_cvt_pk_bf16_f32 v129, v176, v180\n"
      "v_cvt_pk_bf16_f32 v133, v177, v181\n"
      "v_cvt_pk_bf16_f32 v137, v178, v182\n"
      "v_cvt_pk_bf16_f32 v141, v179, v183\n"
      "s_waitcnt vmcnt(2)\n"
      "v_fma_mix_f32 v168, v80, v112, v116 op_sel:[0,0,0] op_sel_hi:[1,0,0]\n"
      "v_fma_mix_f32 v169, v80, v113, v117 op_sel:[1,0,0] op_sel_hi:[1,0,0]\n"
      "v_fma_mix_f32 v170, v81, v114, v118 op_sel:[0,0,0] op_sel_hi:[1,0,0]\n"
      "v_fma_mix_f32 v171, v81, v115, v119 op_sel:[1,0,0] op_sel_hi:[1,0,0]\n"
      "v_fma_mix_f32 v172, v84, v112, v116 op_sel:[0,0,0] op_sel_hi:[1,0,0]\n"
      "v_fma_mix_f32 v173, v84, v113, v117 op_sel:[1,0,0] op_sel_hi:[1,0,0]\n"
      "v_fma_mix_f32 v174, v85, v114, v118 op_sel:[0,0,0] op_sel_hi:[1,0,0]\n"
      "v_fma_mix_f32 v175, v85, v115, v119 op_sel:[1,0,0] op_sel_hi:[1,0,0]\n"
      "v_mul_f32 v168, v124, v168\n"
      "v_mul_f32 v169, v124, v169\n"
      "v_mul_f32 v170, v124, v170\n"
      "v_mul_f32 v171, v124, v171\n"
      "v_lshl_add_u32 v156, s41, 9, v159\n"
      "v_lshl_add_u32 v157, s41, 8, v160\n"
      "buffer_load_format_d16_xyzw v[32:33], v144, s[24:27], s47 offen\n"
      "v_mul_f32 v172, v125, v172\n"
      "v_mul_f32 v173, v125, v173\n"
      "v_mul_f32 v174, v125, v174\n"
      "v_mul_f32 v175, v125, v175\n"
      "buffer_load_format_d16_xyzw v[36:37], v145, s[24:27], s47 offen\n"
      "v_cvt_pk_bf16_f32 v130, v168, v172\n"
      "v_cvt_pk_bf16_f32 v134, v169, v173\n"
      "v_cvt_pk_bf16_f32 v138, v170, v174\n"
      "v_cvt_pk_bf16_f32 v142, v171, v175\n"
      "ds_read_b128 v[96:99], v156\n"
      "buffer_load_format_d16_xyzw v[40:41], v146, s[24:27], s47 offen\n"
      "s_waitcnt vmcnt(3)\n"
      "v_fma_mix_f32 v176, v88, v112, v116 op_sel:[0,0,0] op_sel_hi:[1,0,0]\n"
      "v_fma_mix_f32 v177, v88, v113, v117 op_sel:[1,0,0] op_sel_hi:[1,0,0]\n"
      "v_fma_mix_f32 v178, v89, v114, v118 op_sel:[0,0,0] op_sel_hi:[1,0,0]\n"
      "v_fma_mix_f32 v179, v89, v115, v119 op_sel:[1,0,0] op_sel_hi:[1,0,0]\n"
      "ds_read_b128 v[100:103], v156 offset:8192\n"
      "buffer_load_format_d16_xyzw v[44:45], v147, s[24:27], s47 offen\n"
      "v_fma_mix_f32 v180, v92, v112, v116 op_sel:[0,0,0] op_sel_hi:[1,0,0]\n"
      "v_fma_mix_f32 v181, v92, v113, v117 op_sel:[1,0,0] op_sel_hi:[1,0,0]\n"
      "v_fma_mix_f32 v182, v93, v114, v118 op_sel:[0,0,0] op_sel_hi:[1,0,0]\n"
      "v_fma_mix_f32 v183, v93, v115, v119 op_sel:[1,0,0] op_sel_hi:[1,0,0]\n"
      "ds_read_b128 v[104:107], v157\n"
      "buffer_load_format_d16_xyzw v[48:49], v148, s[24:27], s47 offen\n"
      "v_mul_f32 v176, v126, v176\n"
      "v_mul_f32 v177, v126, v177\n"
      "v_mul_f32 v178, v126, v178\n"
      "v_mul_f32 v179, v126, v179\n"
      "ds_read_b128 v[108:111], v157 offset:16\n"
      "s_lshr_b64 s[48:49], s[48:49], 4\n"
      "buffer_load_format_d16_xyzw v[52:53], v149, s[24:27], s47 offen\n"
      "v_mul_f32 v180, v127, v180\n"
      "v_mul_f32 v181, v127, v181\n"
      "v_mul_f32 v182, v127, v182\n"
      "v_mul_f32 v183, v127, v183\n"
      "s_and_b32 s41, s48, 15\n"
      "s_mul_i32 s43, s41, 917504\n"
      "buffer_load_format_d16_xyzw v[56:57], v150, s[24:27], s47 offen\n"
      "v_cvt_pk_bf16_f32 v131, v176, v180\n"
      "v_cvt_pk_bf16_f32 v135, v177, v181\n"
      "v_cvt_pk_bf16_f32 v139, v178, v182\n"
      "v_cvt_pk_bf16_f32 v143, v179, v183\n"
      "s_add_u32 s46, s46, 1\n"
      "buffer_load_format_d16_xyzw v[60:61], v151, s[24:27], s47 offen\n"
      "s_waitcnt lgkmcnt(0)\n"
      "s_branch L_ploop_%=\n"
      "L_pend_%=:\n"
      "s_barrier\n"
      "s_setprio 2\n"
      "ds_write_b128 v158, v[128:131] offset:0\n"
      "ds_write_b128 v158, v[132:135] offset:2048\n"
      "ds_write_b128 v158, v[136:139] offset:4096\n"
      "ds_write_b128 v158, v[140:143] offset:6144\n"
      "s_waitcnt vmcnt(6)\n"
      "v_fma_mix_f32 v168, v64, v112, v116 op_sel:[0,0,0] op_sel_hi:[1,0,0]\n"
      "v_fma_mix_f32 v169, v64, v113, v117 op_sel:[1,0,0] op_sel_hi:[1,0,0]\n"
      "v_fma_mix_f32 v170, v65, v114, v118 op_sel:[0,0,0] op_sel_hi:[1,0,0]\n"
      "v_fma_mix_f32 v171, v65, v115, v119 op_sel:[1,0,0] op_sel_hi:[1,0,0]\n"
      "v_fma_mix_f32 v172, v68, v112, v116 op_sel:[0,0,0] op_sel_hi:[1,0,0]\n"
      "v_fma_mix_f32 v173, v68, v113, v117 op_sel:[1,0,0] op_sel_hi:[1,0,0]\n"
      "v_fma_mix_f32 v174, v69, v114, v118 op_sel:[0,0,0] op_sel_hi:[1,0,0]\n"
      "v_fma_mix_f32 v175, v69, v115, v119 op_sel:[1,0,0] op_sel_hi:[1,0,0]\n"
      "v_mul_f32 v168, v120, v168\n"
      "v_mul_f32 v169, v120, v169\n"
      "v_mul_f32 v170, v120, v170\n"
      "v_mul_f32 v171, v120, v171\n"
      "v_mul_f32 v172, v121, v172\n"
      "v_mul_f32 v173, v121, v173\n"
      "v_mul_f32 v174, v121, v174\n"
      "v_mul_f32 v175, v121, v175\n"
      "v_cvt_pk_bf16_f32 v128, v168, v172\n"
      "v_cvt_pk_bf16_f32 v132, v169, v173\n"
      "v_cvt_pk_bf16_f32 v136, v170, v174\n"
      "v_cvt_pk_bf16_f32 v140, v171, v175\n"
      "s_waitcnt vmcnt(4)\n"
      "v_fma_mix_f32 v176, v72, v112, v116 op_sel:[0,0,0] op_sel_hi:[1,0,0]\n"
      "v_fma_mix_f32 v177, v72, v113, v117 op_sel:[1,0,0] op_sel_hi:[1,0,0]\n"
      "v_fma_mix_f32 v178, v73, v114, v118 op_sel:[0,0,0] op_sel_hi:[1,0,0]\n"
      "v_fma_mix_f32 v179, v73, v115, v119 op_sel:[1,0,0] op_sel_hi:[1,0,0]\n"
      "v_fma_mix_f32 v180, v76, v112, v116 op_sel:[0,0,0] op_sel_hi:[1,0,0]\n"
      "v_fma_mix_f32 v181, v76, v113, v117 op_sel:[1,0,0] op_sel_hi:[1,0,0]\n"
      "v_fma_mix_f32 v182, v77, v114, v118 op_sel:[0,0,0] op_sel_hi:[1,0,0]\n"
      "v_fma_mix_f32 v183, v77, v115, v119 op_sel:[1,0,0] op_sel_hi:[1,0,0]\n"
      "v_mul_f32 v176, v122, v176\n"
      "v_mul_f32 v177, v122, v177\n"
      "s_waitcnt lgkmcnt(0)\n"
      "s_barrier\n"
      "v_mul_f32 v178, v122, v178\n"
      "v_mul_f32 v179, v122, v179\n"
      "v_mul_f32 v180, v123, v180\n"
      "v_mul_f32 v181, v123, v181\n"
      "v_mul_f32 v182, v123, v182\n"
      "v_mul_f32 v183, v123, v183\n"
      "v_cvt_pk_bf16_f32 v129, v176, v180\n"
      "v_cvt_pk_bf16_f32 v133, v177, v181\n"
      "v_cvt_pk_bf16_f32 v137, v178, v182\n"
      "v_cvt_pk_bf16_f32 v141, v179, v183\n"
      "s_waitcnt vmcnt(2)\n"
      "v_fma_mix_f32 v168, v80, v112, v116 op_sel:[0,0,0] op_sel_hi:[1,0,0]\n"
      "v_fma_mix_f32 v169, v80, v113, v117 op_sel:[1,0,0] op_sel_hi:[1,0,0]\n"
      "v_fma_mix_f32 v170, v81, v114, v118 op_sel:[0,0,0] op_sel_hi:[1,0,0]\n"
      "v_fma_mix_f32 v171, v81, v115, v119 op_sel:[1,0,0] op_sel_hi:[1,0,0]\n"
      "v_fma_mix_f32 v172, v84, v112, v116 op_sel:[0,0,0] op_sel_hi:[1,0,0]\n"
      "v_fma_mix_f32 v173, v84, v113, v117 op_sel:[1,0,0] op_sel_hi:[1,0,0]\n"
      "v_fma_mix_f32 v174, v85, v114, v118 op_sel:[0,0,0] op_sel_hi:[1,0,0]\n"
      "v_fma_mix_f32 v175, v85, v115, v119 op_sel:[1,0,0] op_sel_hi:[1,0,0]\n"
      "v_mul_f32 v168, v124, v168\n"
      "v_mul_f32 v169, v124, v169\n"
      "v_mul_f32 v170, v124, v170\n"
      "v_mul_f32 v171, v124, v171\n"
      "v_mul_f32 v172, v125, v172\n"
      "v_mul_f32 v173, v125, v173\n"
      "v_mul_f32 v174, v125, v174\n"
      "v_mul_f32 v175, v125, v175\n"
      "v_cvt_pk_bf16_f32 v130, v168, v172\n"
      "v_cvt_pk_bf16_f32 v134, v169, v173\n"
      "v_cvt_pk_bf16_f32 v138, v170, v174\n"
      "v_cvt_pk_bf16_f32 v142, v171, v175\n"
      "s_waitcnt vmcnt(0)\n"
      "v_fma_mix_f32 v176, v88, v112, v116 op_sel:[0,0,0] op_sel_hi:[1,0,0]\n"
      "v_fma_mix_f32 v177, v88, v113, v117 op_sel:[1,0,0] op_sel_hi:[1,0,0]\n"
      "v_fma_mix_f32 v178, v89, v114, v118 op_sel:[0,0,0] op_sel_hi:[1,0,0]\n"
      "v_fma_mix_f32 v179, v89, v115, v119 op_sel:[1,0,0] op_sel_hi:[1,0,0]\n"
      "v_fma_mix_f32 v180, v92, v112, v116 op_sel:[0,0,0] op_sel_hi:[1,0,0]\n"
      "v_fma_mix_f32 v181, v92, v113, v117 op_sel:[1,0,0] op_sel_hi:[1,0,0]\n"
      "v_fma_mix_f32 v182, v93, v114, v118 op_sel:[0,0,0] op_sel_hi:[1,0,0]\n"
      "v_fma_mix_f32 v183, v93, v115, v119 op_sel:[1,0,0] op_sel_hi:[1,0,0]\n"
      "v_mul_f32 v176, v126, v176\n"
      "v_mul_f32 v177, v126, v177\n"
      "v_mul_f32 v178, v126, v178\n"
      "v_mul_f32 v179, v126, v179\n"
      "v_mul_f32 v180, v127, v180\n"
      "v_mul_f32 v181, v127, v181\n"
      "v_mul_f32 v182, v127, v182\n"
      "v_mul_f32 v183, v127, v183\n"
      "v_cvt_pk_bf16_f32 v131, v176, v180\n"
      "v_cvt_pk_bf16_f32 v135, v177, v181\n"
      "v_cvt_pk_bf16_f32 v139, v178, v182\n"
      "v_cvt_pk_bf16_f32 v143, v179, v183\n"
      "ds_write_b128 v158, v[128:131] offset:32768\n"
      "buffer_load_dwordx4 v152, s[20:23], s44 offen offset:0 lds\n"
      "ds_write_b128 v158, v[132:135] offset:34816\n"
      "buffer_load_dwordx4 v153, s[20:23], s44 offen offset:1024 lds\n"
      "ds_write_b128 v158, v[136:139] offset:36864\n"
      "buffer_load_dwordx4 v154, s[20:23], s44 offen offset:2048 lds\n"
      "ds_write_b128 v158, v[140:143] offset:38912\n"
      "buffer_load_dwordx4 v155, s[20:23], s44 offen offset:3072 lds\n"
      "s_waitcnt lgkmcnt(0)\n"
      "s_barrier\n"
      "s_setprio 0\n"
      "s_add_u32 m0, s45, 4096\n"
      "s_add_u32 s44, s44, 0x40000\n"
      "buffer_load_dwordx4 v152, s[20:23], s44 offen offset:0 lds\n"
      "buffer_load_dwordx4 v153, s[20:23], s44 offen offset:1024 lds\n"
      "buffer_load_dwordx4 v154, s[20:23], s44 offen offset:2048 lds\n"
      "buffer_load_dwordx4 v155, s[20:23], s44 offen offset:3072 lds\n"
      "s_barrier\n"
      "s_waitcnt vmcnt(0)\n"
      "s_barrier\n"
      "s_endpgm\n"
      ".fill 178, 4, 0xBF800000\n"
      "L_stub_%=:\n"
      "s_branch L_back_%=\n"
      "L_cloop_%=:\n"
      "v_mfma_f32_16x16x32_bf16 v[204:207], v[36:39], v[92:95], v[204:207]\n"
      "v_mfma_f32_16x16x32_bf16 v[204:207], v[32:35], v[88:91], v[204:207]\n"
      "s_waitcnt vmcnt(0) lgkmcnt(0)\n"
      "s_barrier\n"
      "v_mfma_f32_16x16x32_bf16 v[220:223], v[40:43], v[88:91], v[220:223]\n"
      "v_mfma_f32_16x16x32_bf16 v[220:223], v[44:47], v[92:95], v[220:223]\n"
      "v_mfma_f32_16x16x32_bf16 v[216:219], v[44:47], v[84:87], v[216:219]\n"
      "v_mfma_f32_16x16x32_bf16 v[216:219], v[40:43], v[80:83], v[216:219]\n"
      "v_mfma_f32_16x16x32_bf16 v[212:215], v[40:43], v[72:75], v[212:215]\n"
      "v_mfma_f32_16x16x32_bf16 v[212:215], v[44:47], v[76:79], v[212:215]\n"
      "v_mfma_f32_16x16x32_bf16 v[208:211], v[44:47], v[68:71], v[208:211]\n"
      "v_mfma_f32_16x16x32_bf16 v[208:211], v[40:43], v[64:67], v[208:211]\n"
      "v_mfma_f32_16x16x32_bf16 v[224:227], v[48:51], v[64:67], v[224:227]\n"
      "v_mfma_f32_16x16x32_bf16 v[224:227], v[52:55], v[68:71], v[224:227]\n"
      "v_mfma_f32_16x16x32_bf16 v[228:231], v[52:55], v[76:79], v[228:231]\n"
      "v_mfma_f32_16x16x32_bf16 v[228:231], v[48:51], v[72:75], v[228:231]\n"
      "buffer_load_dwordx4 v16, s[20:23], s42 offen offset:0 lds\n"
      "v_mfma_f32_16x16x32_bf16 v[232:235], v[48:51], v[80:83], v[232:235]\n"
      "v_mfma_f32_16x16x32_bf16 v[232:235], v[52:55], v[84:87], v[232:235]\n"
      "v_mfma_f32_16x16x32_bf16 v[236:239], v[52:55], v[92:95], v[236:239]\n"
      "buffer_load_dwordx4 v17, s[20:23], s42 offen offset:1024 lds\n"
      "v_mfma_f32_16x16x32_bf16 v[236:239], v[48:51], v[88:91], v[236:239]\n"
      "v_mfma_f32_16x16x32_bf16 v[252:255], v[56:59], v[88:91], v[252:255]\n"
      "ds_read_b128 v[32:35], v8 offset:32768\n"
      "v_mfma_f32_16x16x32_bf16 v[252:255], v[60:63], v[92:95], v[252:255]\n"
      "buffer_load_dwordx4 v18, s[20:23], s42 offen offset:2048 lds\n"
      "ds_read_b128 v[96:99], v12 offset:16384\n"
      "v_mfma_f32_16x16x32_bf16 v[248:251], v[60:63], v[84:87], v[248:251]\n"
      "ds_read_b128 v[36:39], v9 offset:32768\n"
      "v_mfma_f32_16x16x32_bf16 v[248:251], v[56:59], v[80:83], v[248:251]\n"
      "ds_read_b128 v[100:103], v13 offset:16384\n"
      "v_mfma_f32_16x16x32_bf16 v[244:247], v[56:59], v[72:75], v[244:247]\n"
      "buffer_load_dwordx4 v19, s[20:23], s42 offen offset:3072 lds\n"
      "ds_read_b128 v[104:107], v12 offset:18432\n"
      "v_mfma_f32_16x16x32_bf16 v[244:247], v[60:63], v[76:79], v[244:247]\n"
      "ds_read_b128 v[108:111], v13 offset:18432\n"
      "v_mfma_f32_16x16x32_bf16 v[240:243], v[60:63], v[68:71], v[240:243]\n"
      "ds_read_b128 v[112:115], v12 offset:20480\n"
      "v_mfma_f32_16x16x32_bf16 v[240:243], v[56:59], v[64:67], v[240:243]\n"
      "s_add_u32 m0, s43, 4096\n"
      "s_add_u32 s42, s42, s47\n"
      "buffer_load_dwordx4 v16, s[20:23], s42 offen offset:0 lds\n"
      "ds_read_b128 v[116:119], v13 offset:20480\n"
      "s_waitcnt lgkmcnt(4)\n"
      "v_mfma_f32_16x16x32_bf16 v[128:131], v[32:35], v[96:99], v[128:131]\n"
      "ds_read_b128 v[120:123], v12 offset:22528\n"
      "v_mfma_f32_16x16x32_bf16 v[128:131], v[36:39], v[100:103], v[128:131]\n"
      "ds_read_b128 v[124:127], v13 offset:22528\n"
      "s_waitcnt lgkmcnt(4)\n"
      "v_mfma_f32_16x16x32_bf16 v[132:135], v[36:39], v[108:111], v[132:135]\n"
      "buffer_load_dwordx4 v17, s[20:23], s42 offen offset:1024 lds\n"
      "ds_read_b128 v[40:43], v8 offset:34816\n"
      "v_mfma_f32_16x16x32_bf16 v[132:135], v[32:35], v[104:107], v[132:135]\n"
      "ds_read_b128 v[44:47], v9 offset:34816\n"
      "s_waitcnt lgkmcnt(4)\n"
      "v_mfma_f32_16x16x32_bf16 v[136:139], v[32:35], v[112:115], v[136:139]\n"
      "ds_read_b128 v[48:51], v8 offset:36864\n"
      "v_mfma_f32_16x16x32_bf16 v[136:139], v[36:39], v[116:119], v[136:139]\n"
      "buffer_load_dwordx4 v18, s[20:23], s42 offen offset:2048 lds\n"
      "ds_read_b128 v[52:55], v9 offset:36864\n"
      "s_waitcnt lgkmcnt(4)\n"
      "v_mfma_f32_16x16x32_bf16 v[140:143], v[36:39], v[124:127], v[140:143]\n"
      "ds_read_b128 v[56:59], v8 offset:38912\n"
      "v_mfma_f32_16x16x32_bf16 v[140:143], v[32:35], v[120:123], v[140:143]\n"
      "ds_read_b128 v[60:63], v9 offset:38912\n"
      "s_waitcnt lgkmcnt(4)\n"
      "v_mfma_f32_16x16x32_bf16 v[156:159], v[40:43], v[120:123], v[156:159]\n"
      "buffer_load_dwordx4 v19, s[20:23], s42 offen offset:3072 lds\n"
      "ds_read_b128 v[32:35], v8 offset:40960\n"
      "v_mfma_f32_16x16x32_bf16 v[156:159], v[44:47], v[124:127], v[156:159]\n"
      "ds_read_b128 v[36:39], v9 offset:40960\n"
      "v_mfma_f32_16x16x32_bf16 v[152:155], v[44:47], v[116:119], v[152:155]\n"
      "v_mfma_f32_16x16x32_bf16 v[152:155], v[40:43], v[112:115], v[152:155]\n"
      "v_mfma_f32_16x16x32_bf16 v[148:151], v[40:43], v[104:107], v[148:151]\n"
      "v_mfma_f32_16x16x32_bf16 v[148:151], v[44:47], v[108:111], v[148:151]\n"
      "v_mfma_f32_16x16x32_bf16 v[144:147], v[44:47], v[100:103], v[144:147]\n"
      "v_mfma_f32_16x16x32_bf16 v[144:147], v[40:43], v[96:99], v[144:147]\n"
      "ds_read_b128 v[40:43], v8 offset:43008\n"
      "s_waitcnt lgkmcnt(5)\n"
      "v_mfma_f32_16x16x32_bf16 v[160:163], v[48:51], v[96:99], v[160:163]\n"
      "ds_read_b128 v[44:47], v9 offset:43008\n"
      "v_mfma_f32_16x16x32_bf16 v[160:163], v[52:55], v[100:103], v[160:163]\n"
      "v_mfma_f32_16x16x32_bf16 v[164:167], v[52:55], v[108:111], v[164:167]\n"
      "v_mfma_f32_16x16x32_bf16 v[164:167], v[48:51], v[104:107], v[164:167]\n"
      "v_mfma_f32_16x16x32_bf16 v[168:171], v[48:51], v[112:115], v[168:171]\n"
      "v_mfma_f32_16x16x32_bf16 v[168:171], v[52:55], v[116:119], v[168:171]\n"
      "v_mfma_f32_16x16x32_bf16 v[172:175], v[52:55], v[124:127], v[172:175]\n"
      "s_and_b32 s42, s48, 15\n"
      "v_mfma_f32_16x16x32_bf16 v[172:175], v[48:51], v[120:123], v[172:175]\n"
      "ds_read_b128 v[48:51], v8 offset:45056\n"
      "s_lshr_b64 s[48:49], s[48:49], 4\n"
      "s_waitcnt lgkmcnt(5)\n"
      "v_mfma_f32_16x16x32_bf16 v[188:191], v[56:59], v[120:123], v[188:191]\n"
      "ds_read_b128 v[52:55], v9 offset:45056\n"
      "s_lshl_b32 s42, s42, 7\n"
      "v_mfma_f32_16x16x32_bf16 v[188:191], v[60:63], v[124:127], v[188:191]\n"
      "s_lshl4_add_u32 s42, s35, s42\n"
      "v_mfma_f32_16x16x32_bf16 v[184:187], v[60:63], v[116:119], v[184:187]\n"
      "s_add_u32 s43, s35, 32768\n"
      "v_mfma_f32_16x16x32_bf16 v[184:187], v[56:59], v[112:115], v[184:187]\n"
      "s_mov_b32 m0, s43\n"
      "v_mfma_f32_16x16x32_bf16 v[180:183], v[56:59], v[104:107], v[180:183]\n"
      "s_cmp_eq_u32 s46, 6\n"
      "v_mfma_f32_16x16x32_bf16 v[180:183], v[60:63], v[108:111], v[180:183]\n"
      "s_cselect_b32 s42, s53, s42\n"
      "v_mfma_f32_16x16x32_bf16 v[176:179], v[60:63], v[100:103], v[176:179]\n"
      "s_cselect_b32 s47, 0x40000, s47\n"
      "v_mfma_f32_16x16x32_bf16 v[176:179], v[56:59], v[96:99], v[176:179]\n"
      "ds_read_b128 v[56:59], v8 offset:47104\n"
      "s_waitcnt lgkmcnt(5)\n"
      "v_mfma_f32_16x16x32_bf16 v[192:195], v[32:35], v[96:99], v[192:195]\n"
      "ds_read_b128 v[60:63], v9 offset:47104\n"
      "v_mfma_f32_16x16x32_bf16 v[192:195], v[36:39], v[100:103], v[192:195]\n"
      "v_mfma_f32_16x16x32_bf16 v[196:199], v[36:39], v[108:111], v[196:199]\n"
      "v_mfma_f32_16x16x32_bf16 v[196:199], v[32:35], v[104:107], v[196:199]\n"
      "v_mfma_f32_16x16x32_bf16 v[200:203], v[32:35], v[112:115], v[200:203]\n"
      "v_mfma_f32_16x16x32_bf16 v[200:203], v[36:39], v[116:119], v[200:203]\n"
      "v_mfma_f32_16x16x32_bf16 v[204:207], v[36:39], v[124:127], v[204:207]\n"
      "v_mfma_f32_16x16x32_bf16 v[204:207], v[32:35], v[120:123], v[204:207]\n"
      "s_waitcnt vmcnt(0) lgkmcnt(0)\n"
      "s_barrier\n"
      "v_mfma_f32_16x16x32_bf16 v[220:223], v[40:43], v[120:123], v[220:223]\n"
      "v_mfma_f32_16x16x32_bf16 v[220:223], v[44:47], v[124:127], v[220:223]\n"
      "v_mfma_f32_16x16x32_bf16 v[216:219], v[44:47], v[116:119], v[216:219]\n"
      "v_mfma_f32_16x16x32_bf16 v[216:219], v[40:43], v[112:115], v[216:219]\n"
      "v_mfma_f32_16x16x32_bf16 v[212:215], v[40:43], v[104:107], v[212:215]\n"
      "v_mfma_f32_16x16x32_bf16 v[212:215], v[44:47], v[108:111], v[212:215]\n"
      "v_mfma_f32_16x16x32_bf16 v[208:211], v[44:47], v[100:103], v[208:211]\n"
      "v_mfma_f32_16x16x32_bf16 v[208:211], v[40:43], v[96:99], v[208:211]\n"
      "v_mfma_f32_16x16x32_bf16 v[224:227], v[48:51], v[96:99], v[224:227]\n"
      "v_mfma_f32_16x16x32_bf16 v[224:227], v[52:55], v[100:103], v[224:227]\n"
      "v_mfma_f32_16x16x32_bf16 v[228:231], v[52:55], v[108:111], v[228:231]\n"
      "v_mfma_f32_16x16x32_bf16 v[228:231], v[48:51], v[104:107], v[228:231]\n"
      "buffer_load_dwordx4 v16, s[20:23], s42 offen offset:0 lds\n"
      "v_mfma_f32_16x16x32_bf16 v[232:235], v[48:51], v[112:115], v[232:235]\n"
      "v_mfma_f32_16x16x32_bf16 v[232:235], v[52:55], v[116:119], v[232:235]\n"
      "v_mfma_f32_16x16x32_bf16 v[236:239], v[52:55], v[124:127], v[236:239]\n"
      "buffer_load_dwordx4 v17, s[20:23], s42 offen offset:1024 lds\n"
      "v_mfma_f32_16x16x32_bf16 v[236:239], v[48:51], v[120:123], v[236:239]\n"
      "v_mfma_f32_16x16x32_bf16 v[252:255], v[56:59], v[120:123], v[252:255]\n"
      "ds_read_b128 v[32:35], v8 offset:0\n"
      "v_mfma_f32_16x16x32_bf16 v[252:255], v[60:63], v[124:127], v[252:255]\n"
      "buffer_load_dwordx4 v18, s[20:23], s42 offen offset:2048 lds\n"
      "ds_read_b128 v[64:67], v12 offset:0\n"
      "v_mfma_f32_16x16x32_bf16 v[248:251], v[60:63], v[116:119], v[248:251]\n"
      "ds_read_b128 v[36:39], v9 offset:0\n"
      "v_mfma_f32_16x16x32_bf16 v[248:251], v[56:59], v[112:115], v[248:251]\n"
      "ds_read_b128 v[68:71], v13 offset:0\n"
      "v_mfma_f32_16x16x32_bf16 v[244:247], v[56:59], v[104:107], v[244:247]\n"
      "buffer_load_dwordx4 v19, s[20:23], s42 offen offset:3072 lds\n"
      "ds_read_b128 v[72:75], v12 offset:2048\n"
      "v_mfma_f32_16x16x32_bf16 v[244:247], v[60:63], v[108:111], v[244:247]\n"
      "ds_read_b128 v[76:79], v13 offset:2048\n"
      "v_mfma_f32_16x16x32_bf16 v[240:243], v[60:63], v[100:103], v[240:243]\n"
      "ds_read_b128 v[80:83], v12 offset:4096\n"
      "v_mfma_f32_16x16x32_bf16 v[240:243], v[56:59], v[96:99], v[240:243]\n"
      "s_add_u32 m0, s43, 4096\n"
      "s_add_u32 s42, s42, s47\n"
      "buffer_load_dwordx4 v16, s[20:23], s42 offen offset:0 lds\n"
      "ds_read_b128 v[84:87], v13 offset:4096\n"
      "L_cent_%=:\n"
      "s_waitcnt lgkmcnt(4)\n"
      "v_mfma_f32_16x16x32_bf16 v[128:131], v[32:35], v[64:67], v[128:131]\n"
      "ds_read_b128 v[88:91], v12 offset:6144\n"
      "v_mfma_f32_16x16x32_bf16 v[128:131], v[36:39], v[68:71], v[128:131]\n"
      "ds_read_b128 v[92:95], v13 offset:6144\n"
      "s_waitcnt lgkmcnt(4)\n"
      "v_mfma_f32_16x16x32_bf16 v[132:135], v[36:39], v[76:79], v[132:135]\n"
      "buffer_load_dwordx4 v17, s[20:23], s42 offen offset:1024 lds\n"
      "ds_read_b128 v[40:43], v8 offset:2048\n"
      "v_mfma_f32_16x16x32_bf16 v[132:135], v[32:35], v[72:75], v[132:135]\n"
      "ds_read_b128 v[44:47], v9 offset:2048\n"
      "s_waitcnt lgkmcnt(4)\n"
      "v_mfma_f32_16x16x32_bf16 v[136:139], v[32:35], v[80:83], v[136:139]\n"
      "ds_read_b128 v[48:51], v8 offset:4096\n"
      "v_mfma_f32_16x16x32_bf16 v[136:139], v[36:39], v[84:87], v[136:139]\n"
      "buffer_load_dwordx4 v18, s[20:23], s42 offen offset:2048 lds\n"
      "ds_read_b128 v[52:55], v9 offset:4096\n"
      "s_waitcnt lgkmcnt(4)\n"
      "v_mfma_f32_16x16x32_bf16 v[140:143], v[36:39], v[92:95], v[140:143]\n"
      "ds_read_b128 v[56:59], v8 offset:6144\n"
      "v_mfma_f32_16x16x32_bf16 v[140:143], v[32:35], v[88:91], v[140:143]\n"
      "ds_read_b128 v[60:63], v9 offset:6144\n"
      "s_waitcnt lgkmcnt(4)\n"
      "v_mfma_f32_16x16x32_bf16 v[156:159], v[40:43], v[88:91], v[156:159]\n"
      "buffer_load_dwordx4 v19, s[20:23], s42 offen offset:3072 lds\n"
      "ds_read_b128 v[32:35], v8 offset:8192\n"
      "v_mfma_f32_16x16x32_bf16 v[156:159], v[44:47], v[92:95], v[156:159]\n"
      "ds_read_b128 v[36:39], v9 offset:8192\n"
      "v_mfma_f32_16x16x32_bf16 v[152:155], v[44:47], v[84:87], v[152:155]\n"
      "v_mfma_f32_16x16x32_bf16 v[152:155], v[40:43], v[80:83], v[152:155]\n"
      "v_mfma_f32_16x16x32_bf16 v[148:151], v[40:43], v[72:75], v[148:151]\n"
      "v_mfma_f32_16x16x32_bf16 v[148:151], v[44:47], v[76:79], v[148:151]\n"
      "v_mfma_f32_16x16x32_bf16 v[144:147], v[44:47], v[68:71], v[144:147]\n"
      "v_mfma_f32_16x16x32_bf16 v[144:147], v[40:43], v[64:67], v[144:147]\n"
      "ds_read_b128 v[40:43], v8 offset:10240\n"
      "s_waitcnt lgkmcnt(5)\n"
      "v_mfma_f32_16x16x32_bf16 v[160:163], v[48:51], v[64:67], v[160:163]\n"
      "ds_read_b128 v[44:47], v9 offset:10240\n"
      "v_mfma_f32_16x16x32_bf16 v[160:163], v[52:55], v[68:71], v[160:163]\n"
      "v_mfma_f32_16x16x32_bf16 v[164:167], v[52:55], v[76:79], v[164:167]\n"
      "v_mfma_f32_16x16x32_bf16 v[164:167], v[48:51], v[72:75], v[164:167]\n"
      "v_mfma_f32_16x16x32_bf16 v[168:171], v[48:51], v[80:83], v[168:171]\n"
      "v_mfma_f32_16x16x32_bf16 v[168:171], v[52:55], v[84:87], v[168:171]\n"
      "v_mfma_f32_16x16x32_bf16 v[172:175], v[52:55], v[92:95], v[172:175]\n"
      "s_and_b32 s42, s48, 15\n"
      "v_mfma_f32_16x16x32_bf16 v[172:175], v[48:51], v[88:91], v[172:175]\n"
      "ds_read_b128 v[48:51], v8 offset:12288\n"
      "s_lshr_b64 s[48:49], s[48:49], 4\n"
      "s_waitcnt lgkmcnt(5)\n"
      "v_mfma_f32_16x16x32_bf16 v[188:191], v[56:59], v[88:91], v[188:191]\n"
      "ds_read_b128 v[52:55], v9 offset:12288\n"
      "s_lshl_b32 s42, s42, 7\n"
      "v_mfma_f32_16x16x32_bf16 v[188:191], v[60:63], v[92:95], v[188:191]\n"
      "s_lshl4_add_u32 s42, s35, s42\n"
      "v_mfma_f32_16x16x32_bf16 v[184:187], v[60:63], v[84:87], v[184:187]\n"
      "s_add_u32 s43, s35, 0\n"
      "v_mfma_f32_16x16x32_bf16 v[184:187], v[56:59], v[80:83], v[184:187]\n"
      "s_mov_b32 m0, s43\n"
      "v_mfma_f32_16x16x32_bf16 v[180:183], v[56:59], v[72:75], v[180:183]\n"
      "v_mfma_f32_16x16x32_bf16 v[180:183], v[60:63], v[76:79], v[180:183]\n"
      "v_mfma_f32_16x16x32_bf16 v[176:179], v[60:63], v[68:71], v[176:179]\n"
      "v_mfma_f32_16x16x32_bf16 v[176:179], v[56:59], v[64:67], v[176:179]\n"
      "ds_read_b128 v[56:59], v8 offset:14336\n"
      "s_waitcnt lgkmcnt(5)\n"
      "v_mfma_f32_16x16x32_bf16 v[192:195], v[32:35], v[64:67], v[192:195]\n"
      "ds_read_b128 v[60:63], v9 offset:14336\n"
      "v_mfma_f32_16x16x32_bf16 v[192:195], v[36:39], v[68:71], v[192:195]\n"
      "v_mfma_f32_16x16x32_bf16 v[196:199], v[36:39], v[76:79], v[196:199]\n"
      "v_mfma_f32_16x16x32_bf16 v[196:199], v[32:35], v[72:75], v[196:199]\n"
      "v_mfma_f32_16x16x32_bf16 v[200:203], v[32:35], v[80:83], v[200:203]\n"
      "v_mfma_f32_16x16x32_bf16 v[200:203], v[36:39], v[84:87], v[200:203]\n"
      "s_add_u32 s46, s46, 1\n"
      "s_cmp_lt_u32 s46, 7\n"
      "s_cbranch_scc1 L_cloop_%=\n"
      "s_mov_b32 s42, s54\n"
      "s_add_u32 s43, s35, 65536\n"
      "s_mov_b32 m0, s43\n"
      "v_mfma_f32_16x16x32_bf16 v[204:207], v[36:39], v[92:95], v[204:207]\n"
      "v_mfma_f32_16x16x32_bf16 v[204:207], v[32:35], v[88:91], v[204:207]\n"
      "s_waitcnt vmcnt(0) lgkmcnt(0)\n"
      "s_barrier\n"
      "v_mfma_f32_16x16x32_bf16 v[220:223], v[40:43], v[88:91], v[220:223]\n"
      "v_mfma_f32_16x16x32_bf16 v[220:223], v[44:47], v[92:95], v[220:223]\n"
      "v_mfma_f32_16x16x32_bf16 v[216:219], v[44:47], v[84:87], v[216:219]\n"
      "v_mfma_f32_16x16x32_bf16 v[216:219], v[40:43], v[80:83], v[216:219]\n"
      "v_mfma_f32_16x16x32_bf16 v[212:215], v[40:43], v[72:75], v[212:215]\n"
      "v_mfma_f32_16x16x32_bf16 v[212:215], v[44:47], v[76:79], v[212:215]\n"
      "v_mfma_f32_16x16x32_bf16 v[208:211], v[44:47], v[68:71], v[208:211]\n"
      "buffer_load_dwordx4 v16, s[20:23], s42 offen offset:0 lds\n"
      "v_mfma_f32_16x16x32_bf16 v[208:211], v[40:43], v[64:67], v[208:211]\n"
      "v_mfma_f32_16x16x32_bf16 v[224:227], v[48:51], v[64:67], v[224:227]\n"
      "v_mfma_f32_16x16x32_bf16 v[224:227], v[52:55], v[68:71], v[224:227]\n"
      "buffer_load_dwordx4 v17, s[20:23], s42 offen offset:1024 lds\n"
      "v_mfma_f32_16x16x32_bf16 v[228:231], v[52:55], v[76:79], v[228:231]\n"
      "v_mfma_f32_16x16x32_bf16 v[228:231], v[48:51], v[72:75], v[228:231]\n"
      "v_mfma_f32_16x16x32_bf16 v[232:235], v[48:51], v[80:83], v[232:235]\n"
      "buffer_load_dwordx4 v18, s[20:23], s42 offen offset:2048 lds\n"
      "ds_read_b128 v[32:35], v20 offset:32768\n"
      "v_mfma_f32_16x16x32_bf16 v[232:235], v[52:55], v[84:87], v[232:235]\n"
      "ds_read_b128 v[96:99], v12 offset:16384\n"
      "v_mfma_f32_16x16x32_bf16 v[236:239], v[52:55], v[92:95], v[236:239]\n"
      "ds_read_b128 v[36:39], v21 offset:32768\n"
      "v_mfma_f32_16x16x32_bf16 v[236:239], v[48:51], v[88:91], v[236:239]\n"
      "buffer_load_dwordx4 v19, s[20:23], s42 offen offset:3072 lds\n"
      "ds_read_b128 v[100:103], v13 offset:16384\n"
      "v_mfma_f32_16x16x32_bf16 v[252:255], v[56:59], v[88:91], v[252:255]\n"
      "ds_read_b128 v[104:107], v12 offset:18432\n"
      "v_mfma_f32_16x16x32_bf16 v[252:255], v[60:63], v[92:95], v[252:255]\n"
      "ds_read_b128 v[108:111], v13 offset:18432\n"
      "v_mfma_f32_16x16x32_bf16 v[248:251], v[60:63], v[84:87], v[248:251]\n"
      "s_add_u32 m0, s43, 4096\n"
      "s_add_u32 s42, s42, s47\n"
      "buffer_load_dwordx4 v16, s[20:23], s42 offen offset:0 lds\n"
      "ds_read_b128 v[112:115], v12 offset:20480\n"
      "v_mfma_f32_16x16x32_bf16 v[248:251], v[56:59], v[80:83], v[248:251]\n"
      "ds_read_b128 v[116:119], v13 offset:20480\n"
      "v_mfma_f32_16x16x32_bf16 v[244:247], v[56:59], v[72:75], v[244:247]\n"
      "ds_read_b128 v[120:123], v12 offset:22528\n"
      "v_mfma_f32_16x16x32_bf16 v[244:247], v[60:63], v[76:79], v[244:247]\n"
      "buffer_load_dwordx4 v17, s[20:23], s42 offen offset:1024 lds\n"
      "ds_read_b128 v[124:127], v13 offset:22528\n"
      "v_mfma_f32_16x16x32_bf16 v[240:243], v[60:63], v[68:71], v[240:243]\n"
      "ds_read_b128 v[40:43], v20 offset:34816\n"
      "v_mfma_f32_16x16x32_bf16 v[240:243], v[56:59], v[64:67], v[240:243]\n"
      "ds_read_b128 v[44:47], v21 offset:34816\n"
      "s_barrier\n"
      "s_waitcnt lgkmcnt(8)\n"
      "v_mfma_f32_16x16x32_bf16 v[128:131], v[32:35], v[96:99], v[128:131]\n"
      "buffer_load_dwordx4 v18, s[20:23], s42 offen offset:2048 lds\n"
      "ds_read_b128 v[64:67], v12 offset:0\n"
      "v_mfma_f32_16x16x32_bf16 v[128:131], v[36:39], v[100:103], v[128:131]\n"
      "ds_read_b128 v[48:51], v20 offset:40960\n"
      "s_waitcnt lgkmcnt(8)\n"
      "v_mfma_f32_16x16x32_bf16 v[132:135], v[36:39], v[108:111], v[132:135]\n"
      "ds_read_b128 v[52:55], v21 offset:40960\n"
      "v_mfma_f32_16x16x32_bf16 v[132:135], v[32:35], v[104:107], v[132:135]\n"
      "buffer_load_dwordx4 v19, s[20:23], s42 offen offset:3072 lds\n"
      "ds_read_b128 v[68:71], v13 offset:0\n"
      "s_waitcnt lgkmcnt(8)\n"
      "v_mfma_f32_16x16x32_bf16 v[136:139], v[32:35], v[112:115], v[136:139]\n"
      "ds_read_b128 v[72:75], v12 offset:2048\n"
      "v_mfma_f32_16x16x32_bf16 v[136:139], v[36:39], v[116:119], v[136:139]\n"
      "ds_read_b128 v[76:79], v13 offset:2048\n"
      "s_waitcnt lgkmcnt(8)\n"
      "v_mfma_f32_16x16x32_bf16 v[140:143], v[36:39], v[124:127], v[140:143]\n"
      "ds_read_b128 v[80:83], v12 offset:4096\n"
      "v_mfma_f32_16x16x32_bf16 v[140:143], v[32:35], v[120:123], v[140:143]\n"
      "ds_read_b128 v[84:87], v13 offset:4096\n"
      "s_waitcnt lgkmcnt(8)\n"
      "v_mfma_f32_16x16x32_bf16 v[156:159], v[40:43], v[120:123], v[156:159]\n"
      "ds_read_b128 v[88:91], v12 offset:6144\n"
      "v_mfma_f32_16x16x32_bf16 v[156:159], v[44:47], v[124:127], v[156:159]\n"
      "ds_read_b128 v[56:59], v20 offset:43008\n"
      "v_mfma_f32_16x16x32_bf16 v[152:155], v[44:47], v[116:119], v[152:155]\n"
      "ds_read_b128 v[60:63], v21 offset:43008\n"
      "v_mfma_f32_16x16x32_bf16 v[152:155], v[40:43], v[112:115], v[152:155]\n"
      "ds_read_b128 v[92:95], v13 offset:6144\n"
      "v_mfma_f32_16x16x32_bf16 v[148:151], v[40:43], v[104:107], v[148:151]\n"
      "v_mfma_f32_16x16x32_bf16 v[148:151], v[44:47], v[108:111], v[148:151]\n"
      "v_mfma_f32_16x16x32_bf16 v[144:147], v[44:47], v[100:103], v[144:147]\n"
      "v_mfma_f32_16x16x32_bf16 v[144:147], v[40:43], v[96:99], v[144:147]\n"
      "s_waitcnt lgkmcnt(8)\n"
      "v_mfma_f32_16x16x32_bf16 v[128:131], v[48:51], v[64:67], v[128:131]\n"
      "v_mfma_f32_16x16x32_bf16 v[128:131], v[52:55], v[68:71], v[128:131]\n"
      "ds_read_b128 v[32:35], v20 offset:49152\n"
      "s_waitcnt lgkmcnt(7)\n"
      "v_mfma_f32_16x16x32_bf16 v[132:135], v[52:55], v[76:79], v[132:135]\n"
      "ds_read_b128 v[36:39], v21 offset:49152\n"
      "v_mfma_f32_16x16x32_bf16 v[132:135], v[48:51], v[72:75], v[132:135]\n"
      "s_waitcnt lgkmcnt(6)\n"
      "v_mfma_f32_16x16x32_bf16 v[136:139], v[48:51], v[80:83], v[136:139]\n"
      "v_mfma_f32_16x16x32_bf16 v[136:139], v[52:55], v[84:87], v[136:139]\n"
      "s_waitcnt lgkmcnt(2)\n"
      "v_mfma_f32_16x16x32_bf16 v[140:143], v[52:55], v[92:95], v[140:143]\n"
      "v_mfma_f32_16x16x32_bf16 v[140:143], v[48:51], v[88:91], v[140:143]\n"
      "v_mfma_f32_16x16x32_bf16 v[156:159], v[56:59], v[88:91], v[156:159]\n"
      "v_mfma_f32_16x16x32_bf16 v[156:159], v[60:63], v[92:95], v[156:159]\n"
      "ds_read_b128 v[40:43], v20 offset:51200\n"
      "v_mfma_f32_16x16x32_bf16 v[152:155], v[60:63], v[84:87], v[152:155]\n"
      "ds_read_b128 v[44:47], v21 offset:51200\n"
      "v_mfma_f32_16x16x32_bf16 v[152:155], v[56:59], v[80:83], v[152:155]\n"
      "v_mfma_f32_16x16x32_bf16 v[148:151], v[56:59], v[72:75], v[148:151]\n"
      "v_mfma_f32_16x16x32_bf16 v[148:151], v[60:63], v[76:79], v[148:151]\n"
      "v_mfma_f32_16x16x32_bf16 v[144:147], v[60:63], v[68:71], v[144:147]\n"
      "v_mfma_f32_16x16x32_bf16 v[144:147], v[56:59], v[64:67], v[144:147]\n"
      "s_waitcnt lgkmcnt(0)\n"
      "s_barrier\n"
      "ds_read_b128 v[96:99], v12 offset:32768\n"
      "ds_read_b128 v[100:103], v13 offset:32768\n"
      "ds_read_b128 v[104:107], v12 offset:34816\n"
      "ds_read_b128 v[108:111], v13 offset:34816\n"
      "ds_read_b128 v[112:115], v12 offset:36864\n"
      "ds_read_b128 v[116:119], v13 offset:36864\n"
      "ds_read_b128 v[120:123], v12 offset:38912\n"
      "ds_read_b128 v[124:127], v13 offset:38912\n"
      "s_waitcnt lgkmcnt(6)\n"
      "v_mfma_f32_16x16x32_bf16 v[128:131], v[32:35], v[96:99], v[128:131]\n"
      "v_mfma_f32_16x16x32_bf16 v[128:131], v[36:39], v[100:103], v[128:131]\n"
      "ds_read_b128 v[48:51], v20 offset:57344\n"
      "s_waitcnt lgkmcnt(5)\n"
      "v_mfma_f32_16x16x32_bf16 v[132:135], v[36:39], v[108:111], v[132:135]\n"
      "ds_read_b128 v[52:55], v21 offset:57344\n"
      "v_mfma_f32_16x16x32_bf16 v[132:135], v[32:35], v[104:107], v[132:135]\n"
      "s_waitcnt lgkmcnt(4)\n"
      "v_mfma_f32_16x16x32_bf16 v[136:139], v[32:35], v[112:115], v[136:139]\n"
      "v_mfma_f32_16x16x32_bf16 v[136:139], v[36:39], v[116:119], v[136:139]\n"
      "s_waitcnt lgkmcnt(2)\n"
      "v_mfma_f32_16x16x32_bf16 v[140:143], v[36:39], v[124:127], v[140:143]\n"
      "v_mfma_f32_16x16x32_bf16 v[140:143], v[32:35], v[120:123], v[140:143]\n"
      "v_mfma_f32_16x16x32_bf16 v[156:159], v[40:43], v[120:123], v[156:159]\n"
      "v_mfma_f32_16x16x32_bf16 v[156:159], v[44:47], v[124:127], v[156:159]\n"
      "ds_read_b128 v[56:59], v20 offset:59392\n"
      "v_mfma_f32_16x16x32_bf16 v[152:155], v[44:47], v[116:119], v[152:155]\n"
      "ds_read_b128 v[60:63], v21 offset:59392\n"
      "v_mfma_f32_16x16x32_bf16 v[152:155], v[40:43], v[112:115], v[152:155]\n"
      "v_mfma_f32_16x16x32_bf16 v[148:151], v[40:43], v[104:107], v[148:151]\n"
      "v_mfma_f32_16x16x32_bf16 v[148:151], v[44:47], v[108:111], v[148:151]\n"
      "v_mfma_f32_16x16x32_bf16 v[144:147], v[44:47], v[100:103], v[144:147]\n"
      "v_cvt_pk_bf16_f32 v24, v128, v132\n"
      "v_cvt_pk_bf16_f32 v25, v136, v140\n"
      "v_mfma_f32_16x16x32_bf16 v[144:147], v[40:43], v[96:99], v[144:147]\n"
      "s_waitcnt vmcnt(0)\n"
      "buffer_store_dwordx2 v[24:25], v2, s[28:31], s41 offen sc0 sc1\n"
      "s_barrier\n"
      "s_waitcnt lgkmcnt(2)\n"
      "v_mfma_f32_16x16x32_bf16 v[160:163], v[48:51], v[96:99], v[160:163]\n"
      "v_cvt_pk_bf16_f32 v26, v129, v133\n"
      "v_cvt_pk_bf16_f32 v27, v137, v141\n"
      "v_mfma_f32_16x16x32_bf16 v[160:163], v[52:55], v[100:103], v[160:163]\n"
      "ds_read_b128 v[32:35], v22 offset:0\n"
      "buffer_store_dwordx2 v[26:27], v3, s[28:31], s41 offen sc0 sc1\n"
      "v_mfma_f32_16x16x32_bf16 v[164:167], v[52:55], v[108:111], v[164:167]\n"
      "ds_read_b128 v[36:39], v23 offset:0\n"
      "v_cvt_pk_bf16_f32 v28, v130, v134\n"
      "v_cvt_pk_bf16_f32 v29, v138, v142\n"
      "v_mfma_f32_16x16x32_bf16 v[164:167], v[48:51], v[104:107], v[164:167]\n"
      "buffer_store_dwordx2 v[28:29], v4, s[28:31], s41 offen sc0 sc1\n"
      "v_mfma_f32_16x16x32_bf16 v[168:171], v[48:51], v[112:115], v[168:171]\n"
      "v_cvt_pk_bf16_f32 v30, v131, v135\n"
      "v_cvt_pk_bf16_f32 v31, v139, v143\n"
      "v_mfma_f32_16x16x32_bf16 v[168:171], v[52:55], v[116:119], v[168:171]\n"
      "buffer_store_dwordx2 v[30:31], v10, s[28:31], s41 offen sc0 sc1\n"
      "s_add_u32 s41, s41, 114688\n"
      "v_mfma_f32_16x16x32_bf16 v[172:175], v[52:55], v[124:127], v[172:175]\n"
      "v_cvt_pk_bf16_f32 v24, v144, v148\n"
      "v_cvt_pk_bf16_f32 v25, v152, v156\n"
      "v_mfma_f32_16x16x32_bf16 v[172:175], v[48:51], v[120:123], v[172:175]\n"
      "buffer_store_dwordx2 v[24:25], v2, s[28:31], s41 offen sc0 sc1\n"
      "s_waitcnt lgkmcnt(2)\n"
      "v_mfma_f32_16x16x32_bf16 v[188:191], v[56:59], v[120:123], v[188:191]\n"
      "v_cvt_pk_bf16_f32 v26, v145, v149\n"
      "v_cvt_pk_bf16_f32 v27, v153, v157\n"
      "v_mfma_f32_16x16x32_bf16 v[188:191], v[60:63], v[124:127], v[188:191]\n"
      "ds_read_b128 v[40:43], v22 offset:2048\n"
      "buffer_store_dwordx2 v[26:27], v3, s[28:31], s41 offen sc0 sc1\n"
      "v_mfma_f32_16x16x32_bf16 v[184:187], v[60:63], v[116:119], v[184:187]\n"
      "ds_read_b128 v[44:47], v23 offset:2048\n"
      "v_cvt_pk_bf16_f32 v28, v146, v150\n"
      "v_cvt_pk_bf16_f32 v29, v154, v158\n"
      "v_mfma_f32_16x16x32_bf16 v[184:187], v[56:59], v[112:115], v[184:187]\n"
      "buffer_store_dwordx2 v[28:29], v4, s[28:31], s41 offen sc0 sc1\n"
      "v_mfma_f32_16x16x32_bf16 v[180:183], v[56:59], v[104:107], v[180:183]\n"
      "v_cvt_pk_bf16_f32 v30, v147, v151\n"
      "v_cvt_pk_bf16_f32 v31, v155, v159\n"
      "v_mfma_f32_16x16x32_bf16 v[180:183], v[60:63], v[108:111], v[180:183]\n"
      "buffer_store_dwordx2 v[30:31], v10, s[28:31], s41 offen sc0 sc1\n"
      "s_add_u32 s41, s41, 114688\n"
      "v_mfma_f32_16x16x32_bf16 v[176:179], v[60:63], v[100:103], v[176:179]\n"
      "v_mfma_f32_16x16x32_bf16 v[176:179], v[56:59], v[96:99], v[176:179]\n"
      "s_waitcnt lgkmcnt(2)\n"
      "v_mfma_f32_16x16x32_bf16 v[160:163], v[32:35], v[64:67], v[160:163]\n"
      "ds_read_b128 v[96:99], v12 offset:16384\n"
      "v_mfma_f32_16x16x32_bf16 v[160:163], v[36:39], v[68:71], v[160:163]\n"
      "ds_read_b128 v[48:51], v22 offset:8192\n"
      "v_mfma_f32_16x16x32_bf16 v[164:167], v[36:39], v[76:79], v[164:167]\n"
      "ds_read_b128 v[52:55], v23 offset:8192\n"
      "v_mfma_f32_16x16x32_bf16 v[164:167], v[32:35], v[72:75], v[164:167]\n"
      "ds_read_b128 v[100:103], v13 offset:16384\n"
      "v_mfma_f32_16x16x32_bf16 v[168:171], v[32:35], v[80:83], v[168:171]\n"
      "ds_read_b128 v[104:107], v12 offset:18432\n"
      "v_mfma_f32_16x16x32_bf16 v[168:171], v[36:39], v[84:87], v[168:171]\n"
      "ds_read_b128 v[108:111], v13 offset:18432\n"
      "v_mfma_f32_16x16x32_bf16 v[172:175], v[36:39], v[92:95], v[172:175]\n"
      "ds_read_b128 v[112:115], v12 offset:20480\n"
      "v_mfma_f32_16x16x32_bf16 v[172:175], v[32:35], v[88:91], v[172:175]\n"
      "ds_read_b128 v[116:119], v13 offset:20480\n"
      "s_waitcnt lgkmcnt(8)\n"
      "v_mfma_f32_16x16x32_bf16 v[188:191], v[40:43], v[88:91], v[188:191]\n"
      "ds_read_b128 v[120:123], v12 offset:22528\n"
      "v_mfma_f32_16x16x32_bf16 v[188:191], v[44:47], v[92:95], v[188:191]\n"
      "ds_read_b128 v[56:59], v22 offset:10240\n"
      "v_mfma_f32_16x16x32_bf16 v[184:187], v[44:47], v[84:87], v[184:187]\n"
      "ds_read_b128 v[60:63], v23 offset:10240\n"
      "v_mfma_f32_16x16x32_bf16 v[184:187], v[40:43], v[80:83], v[184:187]\n"
      "ds_read_b128 v[124:127], v13 offset:22528\n"
      "v_mfma_f32_16x16x32_bf16 v[180:183], v[40:43], v[72:75], v[180:183]\n"
      "v_mfma_f32_16x16x32_bf16 v[180:183], v[44:47], v[76:79], v[180:183]\n"
      "v_mfma_f32_16x16x32_bf16 v[176:179], v[44:47], v[68:71], v[176:179]\n"
      "v_mfma_f32_16x16x32_bf16 v[176:179], v[40:43], v[64:67], v[176:179]\n"
      "s_waitcnt lgkmcnt(8)\n"
      "v_mfma_f32_16x16x32_bf16 v[160:163], v[48:51], v[96:99], v[160:163]\n"
      "v_mfma_f32_16x16x32_bf16 v[160:163], v[52:55], v[100:103], v[160:163]\n"
      "ds_read_b128 v[32:35], v22 offset:16384\n"
      "s_waitcnt lgkmcnt(7)\n"
      "v_mfma_f32_16x16x32_bf16 v[164:167], v[52:55], v[108:111], v[164:167]\n"
      "ds_read_b128 v[36:39], v23 offset:16384\n"
      "v_mfma_f32_16x16x32_bf16 v[164:167], v[48:51], v[104:107], v[164:167]\n"
      "s_waitcnt lgkmcnt(6)\n"
      "v_mfma_f32_16x16x32_bf16 v[168:171], v[48:51], v[112:115], v[168:171]\n"
      "v_mfma_f32_16x16x32_bf16 v[168:171], v[52:55], v[116:119], v[168:171]\n"
      "s_waitcnt lgkmcnt(2)\n"
      "v_mfma_f32_16x16x32_bf16 v[172:175], v[52:55], v[124:127], v[172:175]\n"
      "v_mfma_f32_16x16x32_bf16 v[172:175], v[48:51], v[120:123], v[172:175]\n"
      "v_mfma_f32_16x16x32_bf16 v[188:191], v[56:59], v[120:123], v[188:191]\n"
      "v_mfma_f32_16x16x32_bf16 v[188:191], v[60:63], v[124:127], v[188:191]\n"
      "ds_read_b128 v[40:43], v22 offset:18432\n"
      "v_mfma_f32_16x16x32_bf16 v[184:187], v[60:63], v[116:119], v[184:187]\n"
      "ds_read_b128 v[44:47], v23 offset:18432\n"
      "v_mfma_f32_16x16x32_bf16 v[184:187], v[56:59], v[112:115], v[184:187]\n"
      "v_mfma_f32_16x16x32_bf16 v[180:183], v[56:59], v[104:107], v[180:183]\n"
      "v_mfma_f32_16x16x32_bf16 v[180:183], v[60:63], v[108:111], v[180:183]\n"
      "v_mfma_f32_16x16x32_bf16 v[176:179], v[60:63], v[100:103], v[176:179]\n"
      "v_cvt_pk_bf16_f32 v24, v160, v164\n"
      "v_cvt_pk_bf16_f32 v25, v168, v172\n"
      "v_mfma_f32_16x16x32_bf16 v[176:179], v[56:59], v[96:99], v[176:179]\n"
      "buffer_store_dwordx2 v[24:25], v2, s[28:31], s41 offen sc0 sc1\n"
      "s_waitcnt lgkmcnt(2)\n"
      "v_mfma_f32_16x16x32_bf16 v[192:195], v[32:35], v[96:99], v[192:195]\n"
      "v_cvt_pk_bf16_f32 v26, v161, v165\n"
      "v_cvt_pk_bf16_f32 v27, v169, v173\n"
      "v_mfma_f32_16x16x32_bf16 v[192:195], v[36:39], v[100:103], v[192:195]\n"
      "ds_read_b128 v[48:51], v22 offset:24576\n"
      "buffer_store_dwordx2 v[26:27], v3, s[28:31], s41 offen sc0 sc1\n"
      "v_mfma_f32_16x16x32_bf16 v[196:199], v[36:39], v[108:111], v[196:199]\n"
      "ds_read_b128 v[52:55], v23 offset:24576\n"
      "v_cvt_pk_bf16_f32 v28, v162, v166\n"
      "v_cvt_pk_bf16_f32 v29, v170, v174\n"
      "v_mfma_f32_16x16x32_bf16 v[196:199], v[32:35], v[104:107], v[196:199]\n"
      "buffer_store_dwordx2 v[28:29], v4, s[28:31], s41 offen sc0 sc1\n"
      "v_mfma_f32_16x16x32_bf16 v[200:203], v[32:35], v[112:115], v[200:203]\n"
      "v_cvt_pk_bf16_f32 v30, v163, v167\n"
      "v_cvt_pk_bf16_f32 v31, v171, v175\n"
      "v_mfma_f32_16x16x32_bf16 v[200:203], v[36:39], v[116:119], v[200:203]\n"
      "buffer_store_dwordx2 v[30:31], v10, s[28:31], s41 offen sc0 sc1\n"
      "s_add_u32 s41, s41, 114688\n"
      "v_mfma_f32_16x16x32_bf16 v[204:207], v[36:39], v[124:127], v[204:207]\n"
      "v_cvt_pk_bf16_f32 v24, v176, v180\n"
      "v_cvt_pk_bf16_f32 v25, v184, v188\n"
      "v_mfma_f32_16x16x32_bf16 v[204:207], v[32:35], v[120:123], v[204:207]\n"
      "buffer_store_dwordx2 v[24:25], v2, s[28:31], s41 offen sc0 sc1\n"
      "s_waitcnt lgkmcnt(2)\n"
      "v_mfma_f32_16x16x32_bf16 v[220:223], v[40:43], v[120:123], v[220:223]\n"
      "v_cvt_pk_bf16_f32 v26, v177, v181\n"
      "v_cvt_pk_bf16_f32 v27, v185, v189\n"
      "v_mfma_f32_16x16x32_bf16 v[220:223], v[44:47], v[124:127], v[220:223]\n"
      "ds_read_b128 v[56:59], v22 offset:26624\n"
      "buffer_store_dwordx2 v[26:27], v3, s[28:31], s41 offen sc0 sc1\n"
      "v_mfma_f32_16x16x32_bf16 v[216:219], v[44:47], v[116:119], v[216:219]\n"
      "ds_read_b128 v[60:63], v23 offset:26624\n"
      "v_cvt_pk_bf16_f32 v28, v178, v182\n"
      "v_cvt_pk_bf16_f32 v29, v186, v190\n"
      "v_mfma_f32_16x16x32_bf16 v[216:219], v[40:43], v[112:115], v[216:219]\n"
      "buffer_store_dwordx2 v[28:29], v4, s[28:31], s41 offen sc0 sc1\n"
      "v_mfma_f32_16x16x32_bf16 v[212:215], v[40:43], v[104:107], v[212:215]\n"
      "v_cvt_pk_bf16_f32 v30, v179, v183\n"
      "v_cvt_pk_bf16_f32 v31, v187, v191\n"
      "v_mfma_f32_16x16x32_bf16 v[212:215], v[44:47], v[108:111], v[212:215]\n"
      "buffer_store_dwordx2 v[30:31], v10, s[28:31], s41 offen sc0 sc1\n"
      "s_add_u32 s41, s41, 114688\n"
      "v_mfma_f32_16x16x32_bf16 v[208:211], v[44:47], v[100:103], v[208:211]\n"
      "v_mfma_f32_16x16x32_bf16 v[208:211], v[40:43], v[96:99], v[208:211]\n"
      "s_barrier\n"
      "s_waitcnt lgkmcnt(2)\n"
      "v_mfma_f32_16x16x32_bf16 v[192:195], v[48:51], v[64:67], v[192:195]\n"
      "ds_read_b128 v[96:99], v12 offset:32768\n"
      "v_mfma_f32_16x16x32_bf16 v[192:195], v[52:55], v[68:71], v[192:195]\n"
      "ds_read_b128 v[32:35], v20 offset:0\n"
      "v_mfma_f32_16x16x32_bf16 v[196:199], v[52:55], v[76:79], v[196:199]\n"
      "ds_read_b128 v[36:39], v21 offset:0\n"
      "v_mfma_f32_16x16x32_bf16 v[196:199], v[48:51], v[72:75], v[196:199]\n"
      "ds_read_b128 v[100:103], v13 offset:32768\n"
      "v_mfma_f32_16x16x32_bf16 v[200:203], v[48:51], v[80:83], v[200:203]\n"
      "ds_read_b128 v[104:107], v12 offset:34816\n"
      "v_mfma_f32_16x16x32_bf16 v[200:203], v[52:55], v[84:87], v[200:203]\n"
      "ds_read_b128 v[108:111], v13 offset:34816\n"
      "v_mfma_f32_16x16x32_bf16 v[204:207], v[52:55], v[92:95], v[204:207]\n"
      "ds_read_b128 v[112:115], v12 offset:36864\n"
      "v_mfma_f32_16x16x32_bf16 v[204:207], v[48:51], v[88:91], v[204:207]\n"
      "ds_read_b128 v[116:119], v13 offset:36864\n"
      "s_waitcnt lgkmcnt(8)\n"
      "v_mfma_f32_16x16x32_bf16 v[220:223], v[56:59], v[88:91], v[220:223]\n"
      "ds_read_b128 v[120:123], v12 offset:38912\n"
      "v_mfma_f32_16x16x32_bf16 v[220:223], v[60:63], v[92:95], v[220:223]\n"
      "ds_read_b128 v[40:43], v20 offset:2048\n"
      "v_mfma_f32_16x16x32_bf16 v[216:219], v[60:63], v[84:87], v[216:219]\n"
      "ds_read_b128 v[44:47], v21 offset:2048\n"
      "v_mfma_f32_16x16x32_bf16 v[216:219], v[56:59], v[80:83], v[216:219]\n"
      "ds_read_b128 v[124:127], v13 offset:38912\n"
      "v_mfma_f32_16x16x32_bf16 v[212:215], v[56:59], v[72:75], v[212:215]\n"
      "v_mfma_f32_16x16x32_bf16 v[212:215], v[60:63], v[76:79], v[212:215]\n"
      "v_mfma_f32_16x16x32_bf16 v[208:211], v[60:63], v[68:71], v[208:211]\n"
      "v_mfma_f32_16x16x32_bf16 v[208:211], v[56:59], v[64:67], v[208:211]\n"
      "s_waitcnt lgkmcnt(8)\n"
      "v_mfma_f32_16x16x32_bf16 v[192:195], v[32:35], v[96:99], v[192:195]\n"
      "v_mfma_f32_16x16x32_bf16 v[192:195], v[36:39], v[100:103], v[192:195]\n"
      "ds_read_b128 v[48:51], v20 offset:8192\n"
      "s_waitcnt lgkmcnt(7)\n"
      "v_mfma_f32_16x16x32_bf16 v[196:199], v[36:39], v[108:111], v[196:199]\n"
      "ds_read_b128 v[52:55], v21 offset:8192\n"
      "v_mfma_f32_16x16x32_bf16 v[196:199], v[32:35], v[104:107], v[196:199]\n"
      "s_waitcnt lgkmcnt(6)\n"
      "v_mfma_f32_16x16x32_bf16 v[200:203], v[32:35], v[112:115], v[200:203]\n"
      "v_mfma_f32_16x16x32_bf16 v[200:203], v[36:39], v[116:119], v[200:203]\n"
      "s_waitcnt lgkmcnt(2)\n"
      "v_mfma_f32_16x16x32_bf16 v[204:207], v[36:39], v[124:127], v[204:207]\n"
      "v_mfma_f32_16x16x32_bf16 v[204:207], v[32:35], v[120:123], v[204:207]\n"
      "v_mfma_f32_16x16x32_bf16 v[220:223], v[40:43], v[120:123], v[220:223]\n"
      "v_mfma_f32_16x16x32_bf16 v[220:223], v[44:47], v[124:127], v[220:223]\n"
      "ds_read_b128 v[56:59], v20 offset:10240\n"
      "v_mfma_f32_16x16x32_bf16 v[216:219], v[44:47], v[116:119], v[216:219]\n"
      "ds_read_b128 v[60:63], v21 offset:10240\n"
      "v_mfma_f32_16x16x32_bf16 v[216:219], v[40:43], v[112:115], v[216:219]\n"
      "v_mfma_f32_16x16x32_bf16 v[212:215], v[40:43], v[104:107], v[212:215]\n"
      "v_mfma_f32_16x16x32_bf16 v[212:215], v[44:47], v[108:111], v[212:215]\n"
      "v_mfma_f32_16x16x32_bf16 v[208:211], v[44:47], v[100:103], v[208:211]\n"
      "v_cvt_pk_bf16_f32 v24, v192, v196\n"
      "v_cvt_pk_bf16_f32 v25, v200, v204\n"
      "v_mfma_f32_16x16x32_bf16 v[208:211], v[40:43], v[96:99], v[208:211]\n"
      "buffer_store_dwordx2 v[24:25], v2, s[28:31], s41 offen sc0 sc1\n"
      "s_waitcnt lgkmcnt(2)\n"
      "v_mfma_f32_16x16x32_bf16 v[224:227], v[48:51], v[96:99], v[224:227]\n"
      "v_cvt_pk_bf16_f32 v26, v193, v197\n"
      "v_cvt_pk_bf16_f32 v27, v201, v205\n"
      "v_mfma_f32_16x16x32_bf16 v[224:227], v[52:55], v[100:103], v[224:227]\n"
      "ds_read_b128 v[32:35], v20 offset:16384\n"
      "buffer_store_dwordx2 v[26:27], v3, s[28:31], s41 offen sc0 sc1\n"
      "v_mfma_f32_16x16x32_bf16 v[228:231], v[52:55], v[108:111], v[228:231]\n"
      "ds_read_b128 v[36:39], v21 offset:16384\n"
      "v_cvt_pk_bf16_f32 v28, v194, v198\n"
      "v_cvt_pk_bf16_f32 v29, v202, v206\n"
      "v_mfma_f32_16x16x32_bf16 v[228:231], v[48:51], v[104:107], v[228:231]\n"
      "buffer_store_dwordx2 v[28:29], v4, s[28:31], s41 offen sc0 sc1\n"
      "v_mfma_f32_16x16x32_bf16 v[232:235], v[48:51], v[112:115], v[232:235]\n"
      "v_cvt_pk_bf16_f32 v30, v195, v199\n"
      "v_cvt_pk_bf16_f32 v31, v203, v207\n"
      "v_mfma_f32_16x16x32_bf16 v[232:235], v[52:55], v[116:119], v[232:235]\n"
      "buffer_store_dwordx2 v[30:31], v10, s[28:31], s41 offen sc0 sc1\n"
      "s_add_u32 s41, s41, 114688\n"
      "v_mfma_f32_16x16x32_bf16 v[236:239], v[52:55], v[124:127], v[236:239]\n"
      "v_cvt_pk_bf16_f32 v24, v208, v212\n"
      "v_cvt_pk_bf16_f32 v25, v216, v220\n"
      "v_mfma_f32_16x16x32_bf16 v[236:239], v[48:51], v[120:123], v[236:239]\n"
      "buffer_store_dwordx2 v[24:25], v2, s[28:31], s41 offen sc0 sc1\n"
      "s_waitcnt lgkmcnt(2)\n"
      "v_mfma_f32_16x16x32_bf16 v[252:255], v[56:59], v[120:123], v[252:255]\n"
      "v_cvt_pk_bf16_f32 v26, v209, v213\n"
      "v_cvt_pk_bf16_f32 v27, v217, v221\n"
      "v_mfma_f32_16x16x32_bf16 v[252:255], v[60:63], v[124:127], v[252:255]\n"
      "ds_read_b128 v[40:43], v20 offset:18432\n"
      "buffer_store_dwordx2 v[26:27], v3, s[28:31], s41 offen sc0 sc1\n"
      "v_mfma_f32_16x16x32_bf16 v[248:251], v[60:63], v[116:119], v[248:251]\n"
      "ds_read_b128 v[44:47], v21 offset:18432\n"
      "v_cvt_pk_bf16_f32 v28, v210, v214\n"
      "v_cvt_pk_bf16_f32 v29, v218, v222\n"
      "v_mfma_f32_16x16x32_bf16 v[248:251], v[56:59], v[112:115], v[248:251]\n"
      "buffer_store_dwordx2 v[28:29], v4, s[28:31], s41 offen sc0 sc1\n"
      "v_mfma_f32_16x16x32_bf16 v[244:247], v[56:59], v[104:107], v[244:247]\n"
      "v_cvt_pk_bf16_f32 v30, v211, v215\n"
      "v_cvt_pk_bf16_f32 v31, v219, v223\n"
      "v_mfma_f32_16x16x32_bf16 v[244:247], v[60:63], v[108:111], v[244:247]\n"
      "buffer_store_dwordx2 v[30:31], v10, s[28:31], s41 offen sc0 sc1\n"
      "s_add_u32 s41, s41, 114688\n"
      "v_mfma_f32_16x16x32_bf16 v[240:243], v[60:63], v[100:103], v[240:243]\n"
      "v_mfma_f32_16x16x32_bf16 v[240:243], v[56:59], v[96:99], v[240:243]\n"
      "s_waitcnt lgkmcnt(2)\n"
      "v_mfma_f32_16x16x32_bf16 v[224:227], v[32:35], v[64:67], v[224:227]\n"
      "ds_read_b128 v[96:99], v12 offset:16384\n"
      "v_mfma_f32_16x16x32_bf16 v[224:227], v[36:39], v[68:71], v[224:227]\n"
      "ds_read_b128 v[48:51], v20 offset:24576\n"
      "v_mfma_f32_16x16x32_bf16 v[228:231], v[36:39], v[76:79], v[228:231]\n"
      "ds_read_b128 v[52:55], v21 offset:24576\n"
      "v_mfma_f32_16x16x32_bf16 v[228:231], v[32:35], v[72:75], v[228:231]\n"
      "ds_read_b128 v[100:103], v13 offset:16384\n"
      "v_mfma_f32_16x16x32_bf16 v[232:235], v[32:35], v[80:83], v[232:235]\n"
      "ds_read_b128 v[104:107], v12 offset:18432\n"
      "v_mfma_f32_16x16x32_bf16 v[232:235], v[36:39], v[84:87], v[232:235]\n"
      "ds_read_b128 v[108:111], v13 offset:18432\n"
      "v_mfma_f32_16x16x32_bf16 v[236:239], v[36:39], v[92:95], v[236:239]\n"
      "ds_read_b128 v[112:115], v12 offset:20480\n"
      "v_mfma_f32_16x16x32_bf16 v[236:239], v[32:35], v[88:91], v[236:239]\n"
      "ds_read_b128 v[116:119], v13 offset:20480\n"
      "s_waitcnt lgkmcnt(8)\n"
      "v_mfma_f32_16x16x32_bf16 v[252:255], v[40:43], v[88:91], v[252:255]\n"
      "ds_read_b128 v[120:123], v12 offset:22528\n"
      "v_mfma_f32_16x16x32_bf16 v[252:255], v[44:47], v[92:95], v[252:255]\n"
      "ds_read_b128 v[56:59], v20 offset:26624\n"
      "v_mfma_f32_16x16x32_bf16 v[248:251], v[44:47], v[84:87], v[248:251]\n"
      "ds_read_b128 v[60:63], v21 offset:26624\n"
      "v_mfma_f32_16x16x32_bf16 v[248:251], v[40:43], v[80:83], v[248:251]\n"
      "ds_read_b128 v[124:127], v13 offset:22528\n"
      "v_mfma_f32_16x16x32_bf16 v[244:247], v[40:43], v[72:75], v[244:247]\n"
      "v_mfma_f32_16x16x32_bf16 v[244:247], v[44:47], v[76:79], v[244:247]\n"
      "v_mfma_f32_16x16x32_bf16 v[240:243], v[44:47], v[68:71], v[240:243]\n"
      "v_mfma_f32_16x16x32_bf16 v[240:243], v[40:43], v[64:67], v[240:243]\n"
      "s_waitcnt lgkmcnt(8)\n"
      "v_mfma_f32_16x16x32_bf16 v[224:227], v[48:51], v[96:99], v[224:227]\n"
      "v_mfma_f32_16x16x32_bf16 v[224:227], v[52:55], v[100:103], v[224:227]\n"
      "s_waitcnt lgkmcnt(6)\n"
      "v_mfma_f32_16x16x32_bf16 v[228:231], v[52:55], v[108:111], v[228:231]\n"
      "v_mfma_f32_16x16x32_bf16 v[228:231], v[48:51], v[104:107], v[228:231]\n"
      "s_waitcnt lgkmcnt(4)\n"
      "v_mfma_f32_16x16x32_bf16 v[232:235], v[48:51], v[112:115], v[232:235]\n"
      "v_mfma_f32_16x16x32_bf16 v[232:235], v[52:55], v[116:119], v[232:235]\n"
      "s_waitcnt lgkmcnt(0)\n"
      "v_mfma_f32_16x16x32_bf16 v[236:239], v[52:55], v[124:127], v[236:239]\n"
      "v_mfma_f32_16x16x32_bf16 v[236:239], v[48:51], v[120:123], v[236:239]\n"
      "v_mfma_f32_16x16x32_bf16 v[252:255], v[56:59], v[120:123], v[252:255]\n"
      "v_mfma_f32_16x16x32_bf16 v[252:255], v[60:63], v[124:127], v[252:255]\n"
      "v_mfma_f32_16x16x32_bf16 v[248:251], v[60:63], v[116:119], v[248:251]\n"
      "v_mfma_f32_16x16x32_bf16 v[248:251], v[56:59], v[112:115], v[248:251]\n"
      "v_mfma_f32_16x16x32_bf16 v[244:247], v[56:59], v[104:107], v[244:247]\n"
      "v_mfma_f32_16x16x32_bf16 v[244:247], v[60:63], v[108:111], v[244:247]\n"
      "v_mfma_f32_16x16x32_bf16 v[240:243], v[60:63], v[100:103], v[240:243]\n"
      "v_mfma_f32_16x16x32_bf16 v[240:243], v[56:59], v[96:99], v[240:243]\n"
      "s_nop 7\n"
      "v_cvt_pk_bf16_f32 v24, v224, v228\n"
      "v_cvt_pk_bf16_f32 v25, v232, v236\n"
      "buffer_store_dwordx2 v[24:25], v2, s[28:31], s41 offen sc0 sc1\n"
      "v_cvt_pk_bf16_f32 v26, v225, v229\n"
      "v_cvt_pk_bf16_f32 v27, v233, v237\n"
      "buffer_store_dwordx2 v[26:27], v3, s[28:31], s41 offen sc0 sc1\n"
      "v_cvt_pk_bf16_f32 v28, v226, v230\n"
      "v_cvt_pk_bf16_f32 v29, v234, v238\n"
      "buffer_store_dwordx2 v[28:29], v4, s[28:31], s41 offen sc0 sc1\n"
      "v_cvt_pk_bf16_f32 v30, v227, v231\n"
      "v_cvt_pk_bf16_f32 v31, v235, v239\n"
      "buffer_store_dwordx2 v[30:31], v10, s[28:31], s41 offen sc0 sc1\n"
      "s_add_u32 s41, s41, 114688\n"
      "v_cvt_pk_bf16_f32 v24, v240, v244\n"
      "v_cvt_pk_bf16_f32 v25, v248, v252\n"
      "buffer_store_dwordx2 v[24:25], v2, s[28:31], s41 offen sc0 sc1\n"
      "v_cvt_pk_bf16_f32 v26, v241, v245\n"
      "v_cvt_pk_bf16_f32 v27, v249, v253\n"
      "buffer_store_dwordx2 v[26:27], v3, s[28:31], s41 offen sc0 sc1\n"
      "v_cvt_pk_bf16_f32 v28, v242, v246\n"
      "v_cvt_pk_bf16_f32 v29, v250, v254\n"
      "buffer_store_dwordx2 v[28:29], v4, s[28:31], s41 offen sc0 sc1\n"
      "v_cvt_pk_bf16_f32 v30, v243, v247\n"
      "v_cvt_pk_bf16_f32 v31, v251, v255\n"
      "buffer_store_dwordx2 v[30:31], v10, s[28:31], s41 offen sc0 sc1\n"
      "s_add_u32 s41, s41, 114688\n"
      "s_endpgm\n"
      "L_end_%=:\n"
      : [tid] "+v"(tid)
      : [ka] "s"(__builtin_amdgcn_kernarg_segment_ptr()), [bid] "s"(blockIdx.x)
      : "memory", "scc", "vcc", "v1", "v2", "v3", "v4", "v5", "v6", "v7", "v8", "v9", "v10", "v11", "v12", "v13", "v14", "v15", "v16", "v17", "v18", "v19", "v20", "v21", "v22", "v23", "v24", "v25", "v26", "v27", "v28", "v29", "v30", "v31", "v32", "v33", "v34", "v35", "v36", "v37", "v38", "v39", "v40", "v41", "v42", "v43", "v44", "v45", "v46", "v47", "v48", "v49", "v50", "v51", "v52", "v53", "v54", "v55", "v56", "v57", "v58", "v59", "v60", "v61", "v62", "v63", "v64", "v65", "v66", "v67", "v68", "v69", "v70", "v71", "v72", "v73", "v74", "v75", "v76", "v77", "v78", "v79", "v80", "v81", "v82", "v83", "v84", "v85", "v86", "v87", "v88", "v89", "v90", "v91", "v92", "v93", "v94", "v95", "v96", "v97", "v98", "v99", "v100", "v101", "v102", "v103", "v104", "v105", "v106", "v107", "v108", "v109", "v110", "v111", "v112", "v113", "v114", "v115", "v116", "v117", "v118", "v119", "v120", "v121", "v122", "v123", "v124", "v125", "v126", "v127", "v128", "v129", "v130", "v131", "v132", "v133", "v134", "v135", "v136", "v137", "v138", "v139", "v140", "v141", "v142", "v143", "v144", "v145", "v146", "v147", "v148", "v149", "v150", "v151", "v152", "v153", "v154", "v155", "v156", "v157", "v158", "v159", "v160", "v161", "v162", "v163", "v164", "v165", "v166", "v167", "v168", "v169", "v170", "v171", "v172", "v173", "v174", "v175", "v176", "v177", "v178", "v179", "v180", "v181", "v182", "v183", "v184", "v185", "v186", "v187", "v188", "v189", "v190", "v191", "v192", "v193", "v194", "v195", "v196", "v197", "v198", "v199", "v200", "v201", "v202", "v203", "v204", "v205", "v206", "v207", "v208", "v209", "v210", "v211", "v212", "v213", "v214", "v215", "v216", "v217", "v218", "v219", "v220", "v221", "v222", "v223", "v224", "v225", "v226", "v227", "v228", "v229", "v230", "v231", "v232", "v233", "v234", "v235", "v236", "v237", "v238", "v239", "v240", "v241", "v242", "v243", "v244", "v245", "v246", "v247", "v248", "v249", "v250", "v251", "v252", "v253", "v254", "v255", "s4", "s5", "s6", "s7", "s8", "s9", "s10", "s11", "s12", "s13", "s14", "s15", "s16", "s17", "s18", "s19", "s20", "s21", "s22", "s23", "s24", "s25", "s26", "s27", "s28", "s29", "s30", "s31", "s34", "s35", "s36", "s37", "s38", "s39", "s40", "s41", "s42", "s43", "s44", "s45", "s46", "s47", "s48", "s49", "s50", "s51", "s52", "s53", "s54", "s55", "s56", "s57", "s58", "s59", "s60", "s61", "s62", "s63", "s64", "s65", "s66", "s67", "s68", "s69", "s70", "s71", "s72", "s73", "s74", "s75");
}

extern "C" void kernel_launch(void* const* d_in, const int* in_sizes, int n_in, void* d_out, int out_size, void* d_ws, size_t ws_size, hipStream_t stream) {
  (void)hipFuncSetAttribute((const void*)wq_gemm, hipFuncAttributeMaxDynamicSharedMemorySize, 151552);
  wq_gemm<<<256, 512, 151552, stream>>>(d_in[0], d_in[1], d_in[2], d_in[3], d_in[4], d_out);
}
```
